```python
import torch

def _softmax(x, axis=-1):
    return torch.softmax(x, dim=axis)
def _gelu(x):
    return torch.nn.functional.gelu(x, approximate="tanh")
def _silu(x):
    return torch.nn.functional.silu(x)
def _sigmoid(x):
    return torch.sigmoid(x)
def _layernorm(x, axis=-1, eps=1e-5):
    m = x.mean(dim=axis, keepdim=True)
    v = x.var(dim=axis, keepdim=True, unbiased=False)
    return (x - m) / torch.sqrt(v + eps)
def _scatter_add(tgt, idx, src, axis=0):
    return tgt.clone().index_add_(axis, idx, src)
def _segment_sum(src, seg, n):
    out = torch.zeros((n,) + src.shape[1:], dtype=src.dtype)
    return out.index_add_(0, seg, src)
def _pad(x, pw):
    flat = tuple(v for pair in reversed(pw) for v in pair)
    return torch.nn.functional.pad(x, flat)

kernel_name = 'standalone__mixer_s_mlp__prompt'

def setup_inputs():

    _w = (lambda a: (lambda t: t.to(torch.bfloat16) if t.is_floating_point() else t)(torch.as_tensor(a)))
    B, S, D, F = 1, 2048, 512, 1792
    return {
        "x":  _w((lambda *s: torch.randn(*s, dtype=torch.bfloat16) * 0.4)(B, S, D)),
        "w1": _w((lambda *s: torch.randn(*s, dtype=torch.bfloat16) * 3.2)(D, D) * (D ** -0.5)),
        "w2": _w((lambda *s: torch.randn(*s, dtype=torch.bfloat16) * 3.2)(D, D) * (D ** -0.5)),
        "w3": _w((lambda *s: torch.randn(*s, dtype=torch.bfloat16) * 3.2)(D, F) * (D ** -0.5)),
        "w4": _w((lambda *s: torch.randn(*s, dtype=torch.bfloat16) * 0.8)(F, D) * (F ** -0.5)),
    }

def reference(x, w1, w2, w3, w4):
    B, S, D = x.shape
    h = _gelu(_layernorm(x, axis=-1) @ w1)
    c = D // 4
    g0 = _pad(h[:, :, 0*c:1*c], ((0, 0), (1, 0), (0, 0)))[:, :S, :]
    g1 = _pad(h[:, :, 1*c:2*c], ((0, 0), (0, 1), (0, 0)))[:, 1:, :]
    g2 = _pad(h[:, :, 2*c:3*c], ((0, 0), (2, 0), (0, 0)))[:, :S, :]
    g3 = _pad(h[:, :, 3*c:   ], ((0, 0), (0, 2), (0, 0)))[:, 2:, :]
    hs = torch.concatenate([g0, g1, g2, g3], axis=-1) @ w2
    x1 = x + hs
    h2 = _gelu(_layernorm(x1, axis=-1) @ w3) @ w4
    return x1 + h2
```

```cpp
#include <hip/hip_runtime.h>
#include <hip/hip_bf16.h>
typedef short bf16x8 __attribute__((ext_vector_type(8)));
typedef float f32x4 __attribute__((ext_vector_type(4)));
typedef unsigned short u16;
__device__ __forceinline__ float bf2f(u16 b){ return __builtin_bit_cast(float, (unsigned)b<<16); }
__device__ __forceinline__ u16 f2bf(float f){ __hip_bfloat16 h = __float2bfloat16(f); return __builtin_bit_cast(u16,h); }
__device__ __forceinline__ float gelu(float x){
  float u = 0.7978845608028654f*(x+0.044715f*x*x*x);
  float e = __expf(2.f*u);
  float t = 1.f - 2.f/(e+1.f);
  return 0.5f*x*(1.f+t);
}
template<int MODE,int K,int N>
__global__ __launch_bounds__(256) void gemm(const u16* __restrict__ A,const u16* __restrict__ W,const u16* __restrict__ res,u16* __restrict__ out){
  __shared__ u16 As[64][40];
  __shared__ u16 Bs[64][40];
  __shared__ float mean[64], rstd[64];
  int tid=threadIdx.x, lane=tid&63, w=tid>>6;
  int m0=blockIdx.y*64, n0=blockIdx.x*64;
  if(MODE==0||MODE==2){
    for(int r=w;r<64;r+=4){
      float s=0; for(int k=lane;k<K;k+=64) s+=bf2f(A[(m0+r)*K+k]);
      for(int o=32;o;o>>=1) s+=__shfl_xor(s,o);
      float m=s/K; float v=0; for(int k=lane;k<K;k+=64){float d=bf2f(A[(m0+r)*K+k])-m; v+=d*d;}
      for(int o=32;o;o>>=1) v+=__shfl_xor(v,o);
      if(lane==0){mean[r]=m; rstd[r]=rsqrtf(v/K+1e-5f);}
    }
    __syncthreads();
  }
  f32x4 acc[4]={};
  for(int k0=0;k0<K;k0+=32){
    for(int i=tid;i<64*32;i+=256){
      int r=i>>5,c=i&31; int k=k0+c; int row=m0+r; float v;
      if(MODE==1){ int g=k>>7; int sh=(g==0)?-1:(g==1)?1:(g==2)?-2:2; int rr=row+sh; v=(rr>=0&&rr<2048)?bf2f(A[rr*K+k]):0.f; }
      else v=bf2f(A[row*K+k]);
      if(MODE==0||MODE==2) v=(v-mean[r])*rstd[r];
      As[r][c]=f2bf(v);
    }
    for(int i=tid;i<64*32;i+=256){ int kk=i>>6,n=i&63; Bs[n][kk]=W[(k0+kk)*N+n0+n]; }
    __syncthreads();
    bf16x8 a=*(bf16x8*)&As[w*16+(lane&15)][(lane>>4)*8];
    for(int t=0;t<4;t++){ bf16x8 b=*(bf16x8*)&Bs[t*16+(lane&15)][(lane>>4)*8]; acc[t]=__builtin_amdgcn_mfma_f32_16x16x32_bf16(a,b,acc[t],0,0,0);}
    __syncthreads();
  }
  for(int t=0;t<4;t++) for(int j=0;j<4;j++){
    int row=m0+w*16+(lane>>4)*4+j, col=n0+t*16+(lane&15);
    float v=acc[t][j];
    if(MODE==0||MODE==2) v=gelu(v); else v+=bf2f(res[row*N+col]);
    out[row*N+col]=f2bf(v);
  }
}
extern "C" void kernel_launch(void* const* d_in, const int* in_sizes, int n_in,
                              void* d_out, int out_size, void* d_ws, size_t ws_size,
                              hipStream_t stream) {
  const u16* x=(const u16*)d_in[0]; const u16* w1=(const u16*)d_in[1]; const u16* w2=(const u16*)d_in[2];
  const u16* w3=(const u16*)d_in[3]; const u16* w4=(const u16*)d_in[4];
  u16* h=(u16*)d_ws; u16* x1=h+2048*512; u16* h3=x1+2048*512;
  gemm<0,512,512><<<dim3(8,32),256,0,stream>>>(x,w1,nullptr,h);
  gemm<1,512,512><<<dim3(8,32),256,0,stream>>>(h,w2,x,x1);
  gemm<2,512,1792><<<dim3(28,32),256,0,stream>>>(x1,w3,nullptr,h3);
  gemm<3,1792,512><<<dim3(8,32),256,0,stream>>>(h3,w4,x1,(u16*)d_out);
}
```

```cpp
#include <hip/hip_runtime.h>
#include <stdint.h>

typedef unsigned int u32;
typedef unsigned long long u64;
typedef u32 u32x4 __attribute__((ext_vector_type(4)));
typedef u32 u32x2 __attribute__((ext_vector_type(2)));
typedef float f32x4 __attribute__((ext_vector_type(4)));
typedef float f32x2 __attribute__((ext_vector_type(2)));
typedef short s16x4 __attribute__((ext_vector_type(4)));
typedef short s16x8 __attribute__((ext_vector_type(8)));
typedef __bf16 bf16x8 __attribute__((ext_vector_type(8)));
typedef __bf16 bf16x2 __attribute__((ext_vector_type(2)));
#define AS3 __attribute__((address_space(3)))
#define DEV __device__ __forceinline__

#define WS_HS    0u
#define WS_X1    (2u << 20)
#define WS_H3    (4u << 20)
#define WS_ST    (11u << 20)
#define WS_F1    (12u << 20)
#define WS_F2    (WS_F1 + 8192u)
#define WS_F3    (WS_F1 + 16384u)
#define WS_EP    (WS_F1 + 32768u)
#define WS_DBG   (16u << 20)

#define L_X    0u
#define L_W1   65536u
#define L_W2A  131072u
#define L_MISC 159744u
#define L_TOTAL 163840u
DEV constexpr u32 lds_w2(int ks) { return ks < 7 ? L_W2A + ks * 4096u : (ks - 7) * 4096u; }
DEV constexpr u32 lds_w3(int ks) { return ks < 11 ? 40960u + ks * 8192u : (ks - 11) * 8192u; }

#define WAITV(n) asm volatile("s_waitcnt vmcnt(%0)" ::"i"(n) : "memory")
#define WAITL0() asm volatile("s_waitcnt lgkmcnt(0)" ::: "memory")
#define BAR() do { asm volatile("" ::: "memory"); __builtin_amdgcn_s_barrier(); asm volatile("" ::: "memory"); } while (0)
#define PIN(x) asm volatile("" : "+v"(x))

template <int IMM>
DEV void dma16(u32 voff, const char* sbase, u32 lds) {
  asm volatile("s_mov_b32 m0, %2\n\ts_nop 0\n\tglobal_load_lds_dwordx4 %0, %1 offset:%3"
               ::"v"(voff), "s"(sbase), "s"(lds), "i"(IMM) : "memory");
}
template <int IMM>
DEV u32x4 ld4_sc1(u32 voff, const char* sbase) {
  u32x4 d;
  asm volatile("global_load_dwordx4 %0, %1, %2 offset:%3 sc1" : "=v"(d) : "v"(voff), "s"(sbase), "i"(IMM) : "memory");
  return d;
}
template <int IMM>
DEV u32x2 ld2(u32 voff, const char* sbase) {
  u32x2 d;
  asm volatile("global_load_dwordx2 %0, %1, %2 offset:%3" : "=v"(d) : "v"(voff), "s"(sbase), "i"(IMM) : "memory");
  return d;
}
DEV u32x2 ld2_sc1(u32 voff, const char* sbase) {
  u32x2 d;
  asm volatile("global_load_dwordx2 %0, %1, %2 sc1" : "=v"(d) : "v"(voff), "s"(sbase) : "memory");
  return d;
}
template <int IMM>
DEV void st4_sc1(u32 voff, char* sbase, u32x4 d) {
  asm volatile("global_store_dwordx4 %0, %1, %2 offset:%3 sc1\n\ts_nop 1" ::"v"(voff), "v"(d), "s"(sbase), "i"(IMM) : "memory");
}
template <u32 MLO, u32 MHI>
DEV void st2_sc1(u32 voff, char* sbase, u32x2 d) {
  asm volatile("s_mov_b32 exec_lo, %3\n\ts_mov_b32 exec_hi, %4\n\tglobal_store_dwordx2 %0, %1, %2 sc1\n\ts_mov_b64 exec, -1"
               ::"v"(voff), "v"(d), "s"(sbase), "i"(MLO), "i"(MHI) : "memory");
}
template <int IMM>
DEV void st2(u32 voff, char* sbase, u32x2 d) {
  asm volatile("global_store_dwordx2 %0, %1, %2 offset:%3" ::"v"(voff), "v"(d), "s"(sbase), "i"(IMM) : "memory");
}
DEV s16x4 trd(u32 a) { return __builtin_amdgcn_ds_read_tr16_b64_v4i16((AS3 s16x4*)a); }
DEV bf16x8 cat(s16x4 lo, s16x4 hi) {
  s16x8 v = __builtin_shufflevector(lo, hi, 0, 1, 2, 3, 4, 5, 6, 7);
  return __builtin_bit_cast(bf16x8, v);
}
DEV bf16x8 asb(u32x4 v) { return __builtin_bit_cast(bf16x8, v); }
DEV f32x4 mfma(bf16x8 a, bf16x8 b, f32x4 c) { return __builtin_amdgcn_mfma_f32_16x16x32_bf16(a, b, c, 0, 0, 0); }
DEV u32 pk(float a, float b) {
  f32x2 v = {a, b};
  return __builtin_bit_cast(u32, __builtin_convertvector(v, bf16x2));
}
DEV float asf(u32 v) { return __builtin_bit_cast(float, v); }
DEV float blo(u32 v) { return __builtin_bit_cast(float, v << 16); }
DEV float bhi(u32 v) { return __builtin_bit_cast(float, v & 0xffff0000u); }

DEV float gelu_ln(float t, float c1, float c2, float s) {
  float z = t * __builtin_fmaf(t * t, c2, c1);
  float e = __builtin_amdgcn_exp2f(z);
  return t * __builtin_amdgcn_rcpf(__builtin_fmaf(e, s, s));
}
#define GK1 (-2.302208198f)
#define GK2 (-0.1029432397f)

DEV void poll(u32 voff, const char* base, u32 tlo, u32 thi) {
  for (int it = 0; it < (1 << 16); ++it) {
    u32x2 f = ld2_sc1(voff, base);
    asm volatile("s_waitcnt vmcnt(0)" : "+v"(f)::"memory");
    if (__builtin_amdgcn_ballot_w64((f.x != tlo) | (f.y != thi)) == 0) break;
  }
}

#define TS(i)

__global__ __launch_bounds__(256, 1) void mixer(const char* __restrict__ x, const char* __restrict__ w1,
                                                const char* __restrict__ w2, const char* __restrict__ w3,
                                                const char* __restrict__ w4, char* __restrict__ out,
                                                char* __restrict__ ws) {
  const u32 tid = threadIdx.x, lane = tid & 63;
  const u32 w = __builtin_amdgcn_readfirstlane(tid >> 6);
  const u32 b = blockIdx.x, xcd = b & 7, j = b >> 3;
  const u32 rb = xcd * 4 + (j >> 3), cb = j & 7;
  const u32 rb128 = xcd * 2 + (j >> 4), fb = j & 15;
  const u32 t16 = lane & 15, g = lane >> 4, q = (lane >> 2) & 3, pp = lane & 3;
  const u32 lin = lane * 16, zv = 0;
  const u64 epoch = *(const u64*)(ws + WS_EP + rb128 * 128);

  const u32 grp = cb >> 1;
  const int dlt = (grp & 1) ? (int)(1 + (grp >> 1)) : -(int)(1 + (grp >> 1));
  {
    int r = (int)(rb * 64 + w * 16 + (lane >> 2)) + dlt;
    r = r < 0 ? 0 : (r > 2047 ? 2047 : r);
    u32 xo = (u32)r * 1024 + ((pp ^ ((lane & 32) ? 3u : 0u)) << 4);
    u32 lx = L_X + w * 16384;
    u32 row = lane >> 3, s = lane & 7;
    u32 sw1 = ((row >> 1) & 1) | ((w & 1) << 1);
    u32 wo1 = (w * 8 + row) * 1024 + ((((s >> 1) ^ sw1) << 1 | (s & 1)) << 4);
    const char* w1b = w1 + cb * 128;
    u32 lw = L_W1 + w * 1024;
#define S1LD(ks) dma16<(ks) * 64>(xo, x, lx + (ks) * 960); dma16<0>(wo1, w1b + (ks) * 32768, lw + (ks) * 4096);
    S1LD(0) S1LD(1) S1LD(2) S1LD(3) S1LD(4) S1LD(5) S1LD(6) S1LD(7)
    S1LD(8) S1LD(9) S1LD(10) S1LD(11) S1LD(12) S1LD(13) S1LD(14) S1LD(15)
  }
  u32 wo2;
  {
    u32 row = lane >> 3, s = lane & 7;
    wo2 = row * 1024 + ((((s >> 1) ^ ((row >> 1) & 3)) << 1 | (s & 1)) << 4);
  }
  const char* w2b = w2 + cb * 128 + w * 8192;
#pragma unroll
  for (int ks = 0; ks < 7; ++ks) dma16<0>(wo2, w2b + ks * 32768, lds_w2(ks) + w * 1024);
  u32x2 xr[4];
  {
    u32 o = (rb * 64 + w * 16 + t16) * 1024 + cb * 128 + g * 8;
    xr[0] = ld2<0>(o, x); xr[1] = ld2<32>(o, x); xr[2] = ld2<64>(o, x); xr[3] = ld2<96>(o, x);
  }
  const u64 tag = epoch * 0x9E3779B97F4A7C15ull + 0xD1B54A32D192ED03ull;
  const u32 tlo = (u32)tag, thi = (u32)(tag >> 32);
  const u32x2 tagv = {tlo, thi};
  TS(1);

  const u32 cx = (q >> 1) | ((g & 1) << 1);
  const bf16x8 ones = asb((u32x4){0x3f803f80u, 0x3f803f80u, 0x3f803f80u, 0x3f803f80u});
  const f32x4 zero4 = {0.f, 0.f, 0.f, 0.f};

  {
    f32x4 acc[4] = {zero4, zero4, zero4, zero4}, asum = zero4, asq = zero4, acs = zero4;
    u32 xa = L_X + w * 16384 + (t16 * 4 + (g ^ ((t16 & 8) ? 3u : 0u))) * 16;
    u32 a1[4];
#pragma unroll
    for (int t = 0; t < 4; ++t) a1[t] = L_W1 + (8 * g + q) * 128 + ((t ^ cx) << 5) + pp * 8;
    u32 aw = L_W1 + (8 * g + q) * 128 + ((w ^ cx) << 5) + pp * 8;
#pragma unroll
    for (int k2 = 0; k2 < 8; ++k2) {
      WAITV(43 - 4 * (k2 + 1));
      BAR();
#pragma unroll
      for (int kk = 0; kk < 2; ++kk) {
        const int ks = k2 * 2 + kk;
        bf16x8 xf = asb(*(AS3 u32x4*)(xa + ks * 1024));
#pragma unroll
        for (int t = 0; t < 4; ++t) {
          bf16x8 af = cat(trd(a1[t] + ks * 4096), trd(a1[t] + ks * 4096 + 512));
          acc[t] = mfma(af, xf, acc[t]);
        }
        bf16x8 af = cat(trd(aw + ks * 4096), trd(aw + ks * 4096 + 512));
        acs = mfma(af, ones, acs);
        asum = mfma(ones, xf, asum);
        asq = mfma(xf, xf, asq);
      }
    }
    TS(2);
    *(AS3 f32x4*)(L_MISC + (w * 16 + g * 4) * 4) = acs;
    WAITL0();
    BAR();
#pragma unroll
    for (int ks = 7; ks < 16; ++ks) dma16<0>(wo2, w2b + ks * 32768, lds_w2(ks) + w * 1024);
    float sq = (pp == 0) ? asq[0] : (pp == 1) ? asq[1] : (pp == 2) ? asq[2] : asq[3];
    sq = __shfl(sq, (int)(t16 + 16 * q));
    float mean = asum[0] * (1.f / 512.f);
    float var = __builtin_fmaxf(sq * (1.f / 512.f) - mean * mean, 0.f) + 1e-5f;
    float rstd = __builtin_amdgcn_rsqf(var);
    float sd = var * rstd;
    float c1 = GK1 * rstd, c2 = GK2 * rstd * rstd * rstd;
    int sr = (int)(rb * 64 + w * 16 + t16) + dlt;
    bool ok = sr >= 0 && sr < 2048;
    u32 o[8];
#pragma unroll
    for (int t = 0; t < 4; ++t) {
      f32x4 cs = *(AS3 f32x4*)(L_MISC + (t * 16 + g * 4) * 4);
      float v[4];
#pragma unroll
      for (int r = 0; r < 4; ++r) v[r] = gelu_ln(__builtin_fmaf(-mean, cs[r], acc[t][r]), c1, c2, sd);
      o[2 * t] = ok ? pk(v[0], v[1]) : 0u;
      o[2 * t + 1] = ok ? pk(v[2], v[3]) : 0u;
    }
    char* hb = ws + WS_HS + ((rb * 4 + w) * 16 + cb * 2) * 1024;
    st4_sc1<0>(lin, hb, (u32x4){o[0], o[1], o[2], o[3]});
    st4_sc1<1024>(lin, hb, (u32x4){o[4], o[5], o[6], o[7]});
    TS(3);
  }
  const char* w4b = w4 + cb * 128 + w * (448 * 1024);
#pragma unroll
  for (int m = 0; m < 4; ++m) {
    dma16<0>(wo2, w4b + m * 16384, lds_w3(m) + w * 1024);
    dma16<0>(wo2, w4b + m * 16384 + 8192, lds_w3(m) + 4096 + w * 1024);
  }
  WAITV(8);
  st2_sc1<1, 0>(zv, ws + WS_F1 + ((rb * 4 + w) * 8 + cb) * 8, tagv);
  TS(4);
#pragma unroll
  for (int m = 4; m < 11; ++m) {
    dma16<0>(wo2, w4b + m * 16384, lds_w3(m) + w * 1024);
    dma16<0>(wo2, w4b + m * 16384 + 8192, lds_w3(m) + 4096 + w * 1024);
  }
  s16x4 wf[28][4];
  u32 a4[4];
#pragma unroll
  for (int t = 0; t < 4; ++t) a4[t] = w * 1024 + (g >> 1) * 4096 + (4 * (g & 1) + q) * 128 + ((t ^ cx) << 5) + pp * 8;
  WAITV(15);
#pragma unroll
  for (int m = 0; m < 4; ++m)
#pragma unroll
    for (int t = 0; t < 4; ++t) wf[m][t] = trd(a4[t] + lds_w3(m));

  poll((lane & 7) * 8, ws + WS_F1 + (rb * 4 + w) * 64, tlo, thi);
  TS(5);
  u32 x1p[8];
  {
    u32x4 hf[16];
    const char* hb = ws + WS_HS + (rb * 4 + w) * 16384;
#define LDH(i) hf[4 * i] = ld4_sc1<0>(lin, hb + i * 4096); hf[4 * i + 1] = ld4_sc1<1024>(lin, hb + i * 4096); \
               hf[4 * i + 2] = ld4_sc1<2048>(lin, hb + i * 4096); hf[4 * i + 3] = ld4_sc1<3072>(lin, hb + i * 4096);
    LDH(0) LDH(1) LDH(2) LDH(3)
#pragma unroll
    for (int m = 4; m < 11; ++m)
#pragma unroll
      for (int t = 0; t < 4; ++t) wf[m][t] = trd(a4[t] + lds_w3(m));
    BAR();
    u32 a2[4];
#pragma unroll
    for (int t = 0; t < 4; ++t) a2[t] = (4 * g + q) * 128 + ((t ^ cx) << 5) + pp * 8;
    f32x4 acc[4] = {zero4, zero4, zero4, zero4};
#pragma unroll
    for (int ks = 0; ks < 16; ++ks) {
      asm volatile("s_waitcnt vmcnt(%1)" : "+v"(hf[ks]) : "i"(15 - ks) : "memory");
#pragma unroll
      for (int t = 0; t < 4; ++t) {
        bf16x8 af = cat(trd(a2[t] + lds_w2(ks)), trd(a2[t] + lds_w2(ks) + 2048));
        acc[t] = mfma(af, asb(hf[ks]), acc[t]);
      }
    }
    TS(6);
#pragma unroll
    for (int t = 0; t < 4; ++t) {
      PIN(xr[t]);
      x1p[2 * t] = pk(acc[t][0] + blo(xr[t].x), acc[t][1] + bhi(xr[t].x));
      x1p[2 * t + 1] = pk(acc[t][2] + blo(xr[t].y), acc[t][3] + bhi(xr[t].y));
    }
    u32x4 f0 = {x1p[0], x1p[1], x1p[2], x1p[3]}, f1 = {x1p[4], x1p[5], x1p[6], x1p[7]};
    char* xb = ws + WS_X1 + ((rb * 4 + w) * 16 + cb * 2) * 1024;
    st4_sc1<0>(lin, xb, f0);
    st4_sc1<1024>(lin, xb, f1);
    f32x4 s1 = mfma(ones, asb(f0), zero4), s2 = mfma(asb(f0), asb(f0), zero4);
    s1 = mfma(ones, asb(f1), s1);
    s2 = mfma(asb(f1), asb(f1), s2);
    float sq = (pp == 0) ? s2[0] : (pp == 1) ? s2[1] : (pp == 2) ? s2[2] : s2[3];
    u32x2 sv = {__builtin_bit_cast(u32, s1[0]), __builtin_bit_cast(u32, sq)};
    st2_sc1<0x00F0000Fu, 0xF0000F00u>(t16 * 16, ws + WS_ST + (rb * 4 + w) * 1024 + (cb >> 1) * 256 + (cb & 1) * 8, sv);
    TS(7);
  }
  u32 wo3;
  {
    u32 row = lane >> 4, s = lane & 15;
    wo3 = (w * 4 + row) * 3584 + (((s >> 1) ^ (4 * (w & 1) + row)) << 5) + ((s & 1) << 4);
  }
  const char* w3b = w3 + ((fb * 224) & ~63u);
  WAITL0();
#pragma unroll
  for (int ks = 0; ks < 4; ++ks) {
    dma16<0>(wo3, w3b + ks * 114688, lds_w3(ks) + w * 1024);
    dma16<0>(wo3, w3b + ks * 114688 + 57344, lds_w3(ks) + 4096 + w * 1024);
  }
  WAITV(8);
  st2_sc1<1, 0>(zv, ws + WS_F2 + ((rb * 4 + w) * 8 + cb) * 8, tagv);
  TS(8);
  BAR();
#pragma unroll
  for (int ks = 4; ks < 16; ++ks) {
    dma16<0>(wo3, w3b + ks * 114688, lds_w3(ks) + w * 1024);
    dma16<0>(wo3, w3b + ks * 114688 + 57344, lds_w3(ks) + 4096 + w * 1024);
  }
  u32 a3[7];
#pragma unroll
  for (int t = 0; t < 7; ++t) a3[t] = (4 * g + q) * 256 + (((t + (fb & 1)) ^ (4 * (g & 1) + q)) << 5) + pp * 8;
  WAITV(0);
  BAR();
  {
    u32 ac0 = (4 * g + q) * 256 + (((w + (fb & 1)) ^ (4 * (g & 1) + q)) << 5) + pp * 8;
    u32 t1 = w < 3 ? w + 4 : 6;
    u32 ac1 = (4 * g + q) * 256 + (((t1 + (fb & 1)) ^ (4 * (g & 1) + q)) << 5) + pp * 8;
    f32x4 c0 = zero4, c1 = zero4;
#pragma unroll
    for (int ks = 0; ks < 16; ++ks) {
      c0 = mfma(cat(trd(ac0 + lds_w3(ks)), trd(ac0 + lds_w3(ks) + 4096)), ones, c0);
      c1 = mfma(cat(trd(ac1 + lds_w3(ks)), trd(ac1 + lds_w3(ks) + 4096)), ones, c1);
    }
    *(AS3 f32x4*)(L_MISC + 1024 + (w * 16 + g * 4) * 4) = c0;
    *(AS3 f32x4*)(L_MISC + 1024 + (t1 * 16 + g * 4) * 4) = c1;
    WAITL0();
    BAR();
  }
  TS(9);
  const u32 rb3 = rb128 * 2 + (w >> 1), tt3 = (w & 1) * 2;
  poll((lane & 15) * 8, ws + WS_F2 + (rb3 * 4 + tt3) * 64, tlo, thi);
  TS(10);
  {
    const char* sb = ws + WS_ST + (rb3 * 4 + tt3) * 1024;
    u32x4 st0 = ld4_sc1<0>(lin, sb), st1 = ld4_sc1<1024>(lin, sb);
    u32x4 xf[2][16];
    const char* xb = ws + WS_X1 + (rb3 * 4 + tt3) * 16384;
#define LDX(i) \
    xf[0][4 * i] = ld4_sc1<0>(lin, xb + i * 4096);        xf[1][4 * i] = ld4_sc1<0>(lin, xb + 16384 + i * 4096); \
    xf[0][4 * i + 1] = ld4_sc1<1024>(lin, xb + i * 4096); xf[1][4 * i + 1] = ld4_sc1<1024>(lin, xb + 16384 + i * 4096); \
    xf[0][4 * i + 2] = ld4_sc1<2048>(lin, xb + i * 4096); xf[1][4 * i + 2] = ld4_sc1<2048>(lin, xb + 16384 + i * 4096); \
    xf[0][4 * i + 3] = ld4_sc1<3072>(lin, xb + i * 4096); xf[1][4 * i + 3] = ld4_sc1<3072>(lin, xb + 16384 + i * 4096);
    LDX(0) LDX(1) LDX(2) LDX(3)
    f32x4 acc[2][7];
#pragma unroll
    for (int t = 0; t < 7; ++t) acc[0][t] = acc[1][t] = zero4;
#pragma unroll
    for (int ks = 0; ks < 16; ++ks) {
      asm volatile("s_waitcnt vmcnt(%2)" : "+v"(xf[0][ks]), "+v"(xf[1][ks]) : "i"(30 - 2 * ks) : "memory");
#pragma unroll
      for (int t = 0; t < 4; ++t) {
        bf16x8 af = cat(trd(a3[t] + lds_w3(ks)), trd(a3[t] + lds_w3(ks) + 4096));
        acc[0][t] = mfma(af, asb(xf[0][ks]), acc[0][t]);
        acc[1][t] = mfma(af, asb(xf[1][ks]), acc[1][t]);
      }
    }
    TS(11);
    PIN(st0); PIN(st1);
    float mean[2], c1[2], c2[2], sd[2];
#pragma unroll
    for (int i = 0; i < 2; ++i) {
      u32x4 s = i ? st1 : st0;
      float a = asf(s.x) + asf(s.z);
      float c = asf(s.y) + asf(s.w);
      a += __shfl_xor(a, 16); c += __shfl_xor(c, 16);
      a += __shfl_xor(a, 32); c += __shfl_xor(c, 32);
      mean[i] = a * (1.f / 512.f);
      float var = __builtin_fmaxf(c * (1.f / 512.f) - mean[i] * mean[i], 0.f) + 1e-5f;
      float rstd = __builtin_amdgcn_rsqf(var);
      sd[i] = var * rstd;
      c1[i] = GK1 * rstd;
      c2[i] = GK2 * rstd * rstd * rstd;
    }
    char* hb = ws + WS_H3 + ((rb128 * 16 + fb) * 4 + w) * 7168;
    u32 o[2][14];
#define EPI3(t) { f32x4 cs = *(AS3 f32x4*)(L_MISC + 1024 + (t * 16 + g * 4) * 4); \
    _Pragma("unroll") for (int i = 0; i < 2; ++i) { float v[4]; \
      _Pragma("unroll") for (int r = 0; r < 4; ++r) v[r] = gelu_ln(__builtin_fmaf(-mean[i], cs[r], acc[i][t][r]), c1[i], c2[i], sd[i]); \
      o[i][2 * t] = pk(v[0], v[1]); o[i][2 * t + 1] = pk(v[2], v[3]); } }
    EPI3(0) EPI3(1)
    st4_sc1<0>(lin, hb, (u32x4){o[0][0], o[0][1], o[0][2], o[0][3]});
    st4_sc1<1024>(lin, hb, (u32x4){o[1][0], o[1][1], o[1][2], o[1][3]});
    EPI3(2) EPI3(3)
    st4_sc1<2048>(lin, hb, (u32x4){o[0][4], o[0][5], o[0][6], o[0][7]});
    st4_sc1<3072>(lin, hb, (u32x4){o[1][4], o[1][5], o[1][6], o[1][7]});
    char* f3 = ws + WS_F3 + ((((rb128 * 2 + (w >> 1)) * 16 + fb) * 2 + (w & 1)) * 2) * 8;
#pragma unroll
    for (int ks = 0; ks < 16; ++ks) {
      if (ks == 8) {
        WAITV(0);
        st2_sc1<1, 0>(zv, f3, tagv);
      }
#pragma unroll
      for (int t = 4; t < 7; ++t) {
        bf16x8 af = cat(trd(a3[t] + lds_w3(ks)), trd(a3[t] + lds_w3(ks) + 4096));
        acc[0][t] = mfma(af, asb(xf[0][ks]), acc[0][t]);
        acc[1][t] = mfma(af, asb(xf[1][ks]), acc[1][t]);
      }
    }
    TS(12);
    EPI3(4) EPI3(5) EPI3(6)
    st4_sc1<0>(lin, hb + 4096, (u32x4){o[0][8], o[0][9], o[0][10], o[0][11]});
    st4_sc1<1024>(lin, hb + 4096, (u32x4){o[1][8], o[1][9], o[1][10], o[1][11]});
    st4_sc1<2048>(lin, hb + 4096, (u32x4){o[0][12], o[0][13], o[1][12], o[1][13]});
    BAR();
#pragma unroll
    for (int m = 11; m < 15; ++m) {
      dma16<0>(wo2, w4b + m * 16384, (m - 11) * 8192 + w * 1024);
      dma16<0>(wo2, w4b + m * 16384 + 8192, (m - 11) * 8192 + 4096 + w * 1024);
    }
    WAITV(8);
    st2_sc1<1, 0>(zv, f3 + 8, tagv);
    TS(13);
#pragma unroll
    for (int m = 15; m < 28; ++m) {
      dma16<0>(wo2, w4b + m * 16384, (m - 11) * 8192 + w * 1024);
      dma16<0>(wo2, w4b + m * 16384 + 8192, (m - 11) * 8192 + 4096 + w * 1024);
    }
  }
  poll((lane & 15) * 8, ws + WS_F3 + (((rb128 * 2 + (rb & 1)) * 16 + w * 4) * 4) * 8, tlo, thi);
  TS(14);
  {
#pragma unroll
    for (int m = 11; m < 28; ++m)
#pragma unroll
      for (int t = 0; t < 4; ++t) wf[m][t] = trd(a4[t] + (m - 11) * 8192);
    f32x4 acc[4][4];
#pragma unroll
    for (int i = 0; i < 4; ++i)
#pragma unroll
      for (int t = 0; t < 4; ++t) acc[i][t] = zero4;
    const char* hb = ws + WS_H3 + ((rb128 * 16 + w * 4) * 4 + (rb & 1) * 2) * 7168;
    u32x4 cu[2][2];
#pragma unroll
    for (int p = 0; p < 4; ++p) {
      u32x4 hf[2][7];
      const char* pb = hb + p * 28672;
#pragma unroll
      for (int v = 0; v < 2; ++v) {
        hf[v][0] = ld4_sc1<0>(lin, pb + v * 7168); hf[v][1] = ld4_sc1<1024>(lin, pb + v * 7168);
        hf[v][2] = ld4_sc1<2048>(lin, pb + v * 7168); hf[v][3] = ld4_sc1<3072>(lin, pb + v * 7168);
        hf[v][4] = ld4_sc1<0>(lin, pb + v * 7168 + 4096); hf[v][5] = ld4_sc1<1024>(lin, pb + v * 7168 + 4096);
        hf[v][6] = ld4_sc1<2048>(lin, pb + v * 7168 + 4096);
      }
      WAITV(0);
#pragma unroll
      for (int v = 0; v < 2; ++v)
#pragma unroll
        for (int u = 0; u < 7; ++u) PIN(hf[v][u]);
#pragma unroll
      for (int pr = 0; pr < 3; ++pr)
#pragma unroll
        for (int t = 0; t < 4; ++t) {
          bf16x8 af = cat(wf[p * 7 + 2 * pr][t], wf[p * 7 + 2 * pr + 1][t]);
#pragma unroll
          for (int i = 0; i < 4; ++i) acc[i][t] = mfma(af, asb(hf[i >> 1][2 * pr + (i & 1)]), acc[i][t]);
        }
      if ((p & 1) == 0) {
        cu[0][0] = hf[0][6]; cu[1][0] = hf[1][6];
      } else {
        cu[0][1] = hf[0][6]; cu[1][1] = hf[1][6];
#pragma unroll
        for (int t = 0; t < 4; ++t) {
          bf16x8 af = cat(wf[(p - 1) * 7 + 6][t], wf[p * 7 + 6][t]);
#pragma unroll
          for (int i = 0; i < 4; ++i) {
            u32x4 A = cu[i >> 1][0], B = cu[i >> 1][1];
            u32x4 f = (i & 1) ? (u32x4){A.z, A.w, B.z, B.w} : (u32x4){A.x, A.y, B.x, B.y};
            acc[i][t] = mfma(af, asb(f), acc[i][t]);
          }
        }
      }
    }
    BAR();
#pragma unroll
    for (int i = 0; i < 4; ++i) {
      if (i != (int)w) {
        u32 sl = (u32)i * 3 + (w > (u32)i ? w - 1 : w);
#pragma unroll
        for (int t = 0; t < 4; ++t) *(AS3 f32x4*)(sl * 4096 + t * 1024 + lin) = acc[i][t];
      }
    }
    WAITL0();
    BAR();
    f32x4 r[4];
#pragma unroll
    for (int t = 0; t < 4; ++t) {
      r[t] = w == 0 ? acc[0][t] : w == 1 ? acc[1][t] : w == 2 ? acc[2][t] : acc[3][t];
#pragma unroll
      for (int s = 0; s < 3; ++s) r[t] += *(AS3 f32x4*)((w * 3 + s) * 4096 + t * 1024 + lin);
    }
    u32 oo = (rb * 64 + w * 16 + t16) * 1024 + cb * 128 + g * 8;
#pragma unroll
    for (int t = 0; t < 4; ++t) {
      u32x2 d = {pk(r[t][0] + blo(x1p[2 * t]), r[t][1] + bhi(x1p[2 * t])),
                 pk(r[t][2] + blo(x1p[2 * t + 1]), r[t][3] + bhi(x1p[2 * t + 1]))};
      if (t == 0) st2<0>(oo, out, d);
      if (t == 1) st2<32>(oo, out, d);
      if (t == 2) st2<64>(oo, out, d);
      if (t == 3) st2<96>(oo, out, d);
    }
  }
  if (fb == 0 && w == 0) st2_sc1<1, 0>(zv, ws + WS_EP + rb128 * 128, tagv);
}

extern "C" void kernel_launch(void* const* d_in, const int* in_sizes, int n_in, void* d_out, int out_size,
                              void* d_ws, size_t ws_size, hipStream_t stream) {
  (void)hipFuncSetAttribute((const void*)mixer, hipFuncAttributeMaxDynamicSharedMemorySize, L_TOTAL);
  mixer<<<256, 256, L_TOTAL, stream>>>((const char*)d_in[0], (const char*)d_in[1], (const char*)d_in[2],
                                       (const char*)d_in[3], (const char*)d_in[4], (char*)d_out, (char*)d_ws);
}
```

```cpp
#include <hip/hip_runtime.h>
#include <stdint.h>

typedef unsigned int u32;
typedef unsigned long long u64;
typedef u32 u32x4 __attribute__((ext_vector_type(4)));
typedef u32 u32x2 __attribute__((ext_vector_type(2)));
typedef float f32x4 __attribute__((ext_vector_type(4)));
typedef float f32x2 __attribute__((ext_vector_type(2)));
typedef short s16x4 __attribute__((ext_vector_type(4)));
typedef short s16x8 __attribute__((ext_vector_type(8)));
typedef __bf16 bf16x8 __attribute__((ext_vector_type(8)));
typedef __bf16 bf16x2 __attribute__((ext_vector_type(2)));
#define AS3 __attribute__((address_space(3)))
#define DEV __device__ __forceinline__

#define WS_HS    0u
#define WS_X1    (2u << 20)
#define WS_H3    (4u << 20)
#define WS_ST    (11u << 20)
#define WS_F1    (12u << 20)
#define WS_F2    (WS_F1 + 8192u)
#define WS_F3    (WS_F1 + 16384u)
#define WS_EP    (WS_F1 + 32768u)
#define WS_DBG   (16u << 20)

#define L_X    0u
#define L_W1   65536u
#define L_W2A  131072u
#define L_MISC 159744u
#define L_TOTAL 163840u
DEV constexpr u32 lds_w2(int ks) { return ks < 7 ? L_W2A + ks * 4096u : (ks - 7) * 4096u; }
DEV constexpr u32 lds_w3(int ks) { return ks < 11 ? 40960u + ks * 8192u : (ks - 11) * 8192u; }

#define WAITV(n) asm volatile("s_waitcnt vmcnt(%0)" ::"i"(n) : "memory")
#define WAITL0() asm volatile("s_waitcnt lgkmcnt(0)" ::: "memory")
#define BAR() do { asm volatile("" ::: "memory"); __builtin_amdgcn_s_barrier(); asm volatile("" ::: "memory"); } while (0)
#define PIN(x) asm volatile("" : "+v"(x))

template <int IMM>
DEV void dma16(u32 voff, const char* sbase, u32 lds) {
  asm volatile("s_mov_b32 m0, %2\n\ts_nop 0\n\tglobal_load_lds_dwordx4 %0, %1 offset:%3"
               ::"v"(voff), "s"(sbase), "s"(lds), "i"(IMM) : "memory");
}
template <int IMM>
DEV u32x4 ld4_sc1(u32 voff, const char* sbase) {
  u32x4 d;
  asm volatile("global_load_dwordx4 %0, %1, %2 offset:%3 sc1" : "=v"(d) : "v"(voff), "s"(sbase), "i"(IMM) : "memory");
  return d;
}
template <int IMM>
DEV u32x2 ld2(u32 voff, const char* sbase) {
  u32x2 d;
  asm volatile("global_load_dwordx2 %0, %1, %2 offset:%3" : "=v"(d) : "v"(voff), "s"(sbase), "i"(IMM) : "memory");
  return d;
}
DEV u32x2 ld2_sc1(u32 voff, const char* sbase) {
  u32x2 d;
  asm volatile("global_load_dwordx2 %0, %1, %2 sc1" : "=v"(d) : "v"(voff), "s"(sbase) : "memory");
  return d;
}
template <int IMM>
DEV void st4_sc1(u32 voff, char* sbase, u32x4 d) {
  asm volatile("global_store_dwordx4 %0, %1, %2 offset:%3 sc1\n\ts_nop 1" ::"v"(voff), "v"(d), "s"(sbase), "i"(IMM) : "memory");
}
template <u32 MLO, u32 MHI>
DEV void st2_sc1(u32 voff, char* sbase, u32x2 d) {
  asm volatile("s_mov_b32 exec_lo, %3\n\ts_mov_b32 exec_hi, %4\n\tglobal_store_dwordx2 %0, %1, %2 sc1\n\ts_mov_b64 exec, -1"
               ::"v"(voff), "v"(d), "s"(sbase), "i"(MLO), "i"(MHI) : "memory");
}
template <int IMM>
DEV void st2(u32 voff, char* sbase, u32x2 d) {
  asm volatile("global_store_dwordx2 %0, %1, %2 offset:%3" ::"v"(voff), "v"(d), "s"(sbase), "i"(IMM) : "memory");
}
DEV s16x4 trd(u32 a) { return __builtin_amdgcn_ds_read_tr16_b64_v4i16((AS3 s16x4*)a); }
DEV bf16x8 cat(s16x4 lo, s16x4 hi) {
  s16x8 v = __builtin_shufflevector(lo, hi, 0, 1, 2, 3, 4, 5, 6, 7);
  return __builtin_bit_cast(bf16x8, v);
}
DEV bf16x8 asb(u32x4 v) { return __builtin_bit_cast(bf16x8, v); }
DEV f32x4 mfma(bf16x8 a, bf16x8 b, f32x4 c) { return __builtin_amdgcn_mfma_f32_16x16x32_bf16(a, b, c, 0, 0, 0); }
DEV u32 pk(float a, float b) {
  f32x2 v = {a, b};
  return __builtin_bit_cast(u32, __builtin_convertvector(v, bf16x2));
}
DEV float asf(u32 v) { return __builtin_bit_cast(float, v); }
DEV float blo(u32 v) { return __builtin_bit_cast(float, v << 16); }
DEV float bhi(u32 v) { return __builtin_bit_cast(float, v & 0xffff0000u); }

DEV float gelu_ln(float t, float c1, float c2, float s) {
  float z = t * __builtin_fmaf(t * t, c2, c1);
  float e = __builtin_amdgcn_exp2f(z);
  return t * __builtin_amdgcn_rcpf(__builtin_fmaf(e, s, s));
}
#define GK1 (-2.302208198f)
#define GK2 (-0.1029432397f)

DEV void poll(u32 voff, const char* base, u32 tlo, u32 thi) {
  for (int it = 0; it < (1 << 16); ++it) {
    u32x2 f = ld2_sc1(voff, base);
    asm volatile("s_waitcnt vmcnt(0)" : "+v"(f)::"memory");
    if (__builtin_amdgcn_ballot_w64((f.x != tlo) | (f.y != thi)) == 0) break;
  }
}

#define TS(i)

__global__ __launch_bounds__(256, 1) void mixer(const char* __restrict__ x, const char* __restrict__ w1,
                                                const char* __restrict__ w2, const char* __restrict__ w3,
                                                const char* __restrict__ w4, char* __restrict__ out,
                                                char* __restrict__ ws) {
  const u64 t00 = __builtin_amdgcn_s_memrealtime();
  const u32 tid = threadIdx.x, lane = tid & 63;
  const u32 w = __builtin_amdgcn_readfirstlane(tid >> 6);
  const u32 b = blockIdx.x, xcd = b & 7, j = b >> 3;
  const u32 rb = xcd * 4 + (j >> 3), cb = j & 7;
  const u32 rb128 = xcd * 2 + (j >> 4), fb = j & 15;
  const u32 t16 = lane & 15, g = lane >> 4, q = (lane >> 2) & 3, pp = lane & 3;
  const u32 lin = lane * 16, zv = 0;
  const u64 epoch = *(const u64*)(ws + WS_EP + rb128 * 128);

  const u32 grp = cb >> 1;
  const int dlt = (grp & 1) ? (int)(1 + (grp >> 1)) : -(int)(1 + (grp >> 1));
  {
    int r = (int)(rb * 64 + w * 16 + (lane >> 2)) + dlt;
    r = r < 0 ? 0 : (r > 2047 ? 2047 : r);
    u32 xo = (u32)r * 1024 + ((pp ^ ((lane & 32) ? 3u : 0u)) << 4);
    u32 lx = L_X + w * 16384;
    u32 row = lane >> 3, s = lane & 7;
    u32 sw1 = ((row >> 1) & 1) | ((w & 1) << 1);
    u32 wo1 = (w * 8 + row) * 1024 + ((((s >> 1) ^ sw1) << 1 | (s & 1)) << 4);
    const char* w1b = w1 + cb * 128;
    u32 lw = L_W1 + w * 1024;
#define S1LD(ks) dma16<(ks) * 64>(xo, x, lx + (ks) * 960); dma16<0>(wo1, w1b + (ks) * 32768, lw + (ks) * 4096);
    S1LD(0) S1LD(1) S1LD(2) S1LD(3) S1LD(4) S1LD(5) S1LD(6) S1LD(7)
    S1LD(8) S1LD(9) S1LD(10) S1LD(11) S1LD(12) S1LD(13) S1LD(14) S1LD(15)
  }
  u32 wo2;
  {
    u32 row = lane >> 3, s = lane & 7;
    wo2 = row * 1024 + ((((s >> 1) ^ ((row >> 1) & 3)) << 1 | (s & 1)) << 4);
  }
  const char* w2b = w2 + cb * 128 + w * 8192;
#pragma unroll
  for (int ks = 0; ks < 7; ++ks) dma16<0>(wo2, w2b + ks * 32768, lds_w2(ks) + w * 1024);
  u32x2 xr[4];
  {
    u32 o = (rb * 64 + w * 16 + t16) * 1024 + cb * 128 + g * 8;
    xr[0] = ld2<0>(o, x); xr[1] = ld2<32>(o, x); xr[2] = ld2<64>(o, x); xr[3] = ld2<96>(o, x);
  }
  const u64 tag = epoch * 0x9E3779B97F4A7C15ull + 0xD1B54A32D192ED03ull;
  const u32 tlo = (u32)tag, thi = (u32)(tag >> 32);
  const u32x2 tagv = {tlo, thi};
  { u32x4 rec = {(u32)t00, (u32)(t00 >> 32), 0x1234567u, 0x89abcdu}; if (tid == 0) *(volatile u32x4*)(ws + (20u << 20) + b * 16) = rec; __threadfence(); }
  TS(1);

  const u32 cx = (q >> 1) | ((g & 1) << 1);
  const bf16x8 ones = asb((u32x4){0x3f803f80u, 0x3f803f80u, 0x3f803f80u, 0x3f803f80u});
  const f32x4 zero4 = {0.f, 0.f, 0.f, 0.f};

  {
    f32x4 acc[4] = {zero4, zero4, zero4, zero4}, asum = zero4, asq = zero4, acs = zero4;
    u32 xa = L_X + w * 16384 + (t16 * 4 + (g ^ ((t16 & 8) ? 3u : 0u))) * 16;
    u32 a1[4];
#pragma unroll
    for (int t = 0; t < 4; ++t) a1[t] = L_W1 + (8 * g + q) * 128 + ((t ^ cx) << 5) + pp * 8;
    u32 aw = L_W1 + (8 * g + q) * 128 + ((w ^ cx) << 5) + pp * 8;
#pragma unroll
    for (int k2 = 0; k2 < 8; ++k2) {
      WAITV(43 - 4 * (k2 + 1));
      BAR();
#pragma unroll
      for (int kk = 0; kk < 2; ++kk) {
        const int ks = k2 * 2 + kk;
        bf16x8 xf = asb(*(AS3 u32x4*)(xa + ks * 1024));
#pragma unroll
        for (int t = 0; t < 4; ++t) {
          bf16x8 af = cat(trd(a1[t] + ks * 4096), trd(a1[t] + ks * 4096 + 512));
          acc[t] = mfma(af, xf, acc[t]);
        }
        bf16x8 af = cat(trd(aw + ks * 4096), trd(aw + ks * 4096 + 512));
        acs = mfma(af, ones, acs);
        asum = mfma(ones, xf, asum);
        asq = mfma(xf, xf, asq);
      }
    }
    TS(2);
    *(AS3 f32x4*)(L_MISC + (w * 16 + g * 4) * 4) = acs;
    WAITL0();
    BAR();
#pragma unroll
    for (int ks = 7; ks < 16; ++ks) dma16<0>(wo2, w2b + ks * 32768, lds_w2(ks) + w * 1024);
    float sq = (pp == 0) ? asq[0] : (pp == 1) ? asq[1] : (pp == 2) ? asq[2] : asq[3];
    sq = __shfl(sq, (int)(t16 + 16 * q));
    float mean = asum[0] * (1.f / 512.f);
    float var = __builtin_fmaxf(sq * (1.f / 512.f) - mean * mean, 0.f) + 1e-5f;
    float rstd = __builtin_amdgcn_rsqf(var);
    float sd = var * rstd;
    float c1 = GK1 * rstd, c2 = GK2 * rstd * rstd * rstd;
    int sr = (int)(rb * 64 + w * 16 + t16) + dlt;
    bool ok = sr >= 0 && sr < 2048;
    u32 o[8];
#pragma unroll
    for (int t = 0; t < 4; ++t) {
      f32x4 cs = *(AS3 f32x4*)(L_MISC + (t * 16 + g * 4) * 4);
      float v[4];
#pragma unroll
      for (int r = 0; r < 4; ++r) v[r] = gelu_ln(__builtin_fmaf(-mean, cs[r], acc[t][r]), c1, c2, sd);
      o[2 * t] = ok ? pk(v[0], v[1]) : 0u;
      o[2 * t + 1] = ok ? pk(v[2], v[3]) : 0u;
    }
    char* hb = ws + WS_HS + ((rb * 4 + w) * 16 + cb * 2) * 1024;
    st4_sc1<0>(lin, hb, (u32x4){o[0], o[1], o[2], o[3]});
    st4_sc1<1024>(lin, hb, (u32x4){o[4], o[5], o[6], o[7]});
    TS(3);
  }
  const char* w4b = w4 + cb * 128 + w * (448 * 1024);
#pragma unroll
  for (int m = 0; m < 4; ++m) {
    dma16<0>(wo2, w4b + m * 16384, lds_w3(m) + w * 1024);
    dma16<0>(wo2, w4b + m * 16384 + 8192, lds_w3(m) + 4096 + w * 1024);
  }
  WAITV(8);
  st2_sc1<1, 0>(zv, ws + WS_F1 + ((rb * 4 + w) * 8 + cb) * 8, tagv);
  TS(4);
#pragma unroll
  for (int m = 4; m < 11; ++m) {
    dma16<0>(wo2, w4b + m * 16384, lds_w3(m) + w * 1024);
    dma16<0>(wo2, w4b + m * 16384 + 8192, lds_w3(m) + 4096 + w * 1024);
  }
  s16x4 wf[28][4];
  u32 a4[4];
#pragma unroll
  for (int t = 0; t < 4; ++t) a4[t] = w * 1024 + (g >> 1) * 4096 + (4 * (g & 1) + q) * 128 + ((t ^ cx) << 5) + pp * 8;
  WAITV(15);
#pragma unroll
  for (int m = 0; m < 4; ++m)
#pragma unroll
    for (int t = 0; t < 4; ++t) wf[m][t] = trd(a4[t] + lds_w3(m));

  poll((lane & 7) * 8, ws + WS_F1 + (rb * 4 + w) * 64, tlo, thi);
  TS(5);
  u32 x1p[8];
  {
    u32x4 hf[16];
    const char* hb = ws + WS_HS + (rb * 4 + w) * 16384;
#define LDH(i) hf[4 * i] = ld4_sc1<0>(lin, hb + i * 4096); hf[4 * i + 1] = ld4_sc1<1024>(lin, hb + i * 4096); \
               hf[4 * i + 2] = ld4_sc1<2048>(lin, hb + i * 4096); hf[4 * i + 3] = ld4_sc1<3072>(lin, hb + i * 4096);
    LDH(0) LDH(1) LDH(2) LDH(3)
#pragma unroll
    for (int m = 4; m < 11; ++m)
#pragma unroll
      for (int t = 0; t < 4; ++t) wf[m][t] = trd(a4[t] + lds_w3(m));
    BAR();
    u32 a2[4];
#pragma unroll
    for (int t = 0; t < 4; ++t) a2[t] = (4 * g + q) * 128 + ((t ^ cx) << 5) + pp * 8;
    f32x4 acc[4] = {zero4, zero4, zero4, zero4};
#pragma unroll
    for (int ks = 0; ks < 16; ++ks) {
      asm volatile("s_waitcnt vmcnt(%1)" : "+v"(hf[ks]) : "i"(15 - ks) : "memory");
#pragma unroll
      for (int t = 0; t < 4; ++t) {
        bf16x8 af = cat(trd(a2[t] + lds_w2(ks)), trd(a2[t] + lds_w2(ks) + 2048));
        acc[t] = mfma(af, asb(hf[ks]), acc[t]);
      }
    }
    TS(6);
#pragma unroll
    for (int t = 0; t < 4; ++t) {
      PIN(xr[t]);
      x1p[2 * t] = pk(acc[t][0] + blo(xr[t].x), acc[t][1] + bhi(xr[t].x));
      x1p[2 * t + 1] = pk(acc[t][2] + blo(xr[t].y), acc[t][3] + bhi(xr[t].y));
    }
    u32x4 f0 = {x1p[0], x1p[1], x1p[2], x1p[3]}, f1 = {x1p[4], x1p[5], x1p[6], x1p[7]};
    char* xb = ws + WS_X1 + ((rb * 4 + w) * 16 + cb * 2) * 1024;
    st4_sc1<0>(lin, xb, f0);
    st4_sc1<1024>(lin, xb, f1);
    f32x4 s1 = mfma(ones, asb(f0), zero4), s2 = mfma(asb(f0), asb(f0), zero4);
    s1 = mfma(ones, asb(f1), s1);
    s2 = mfma(asb(f1), asb(f1), s2);
    float sq = (pp == 0) ? s2[0] : (pp == 1) ? s2[1] : (pp == 2) ? s2[2] : s2[3];
    u32x2 sv = {__builtin_bit_cast(u32, s1[0]), __builtin_bit_cast(u32, sq)};
    st2_sc1<0x00F0000Fu, 0xF0000F00u>(t16 * 16, ws + WS_ST + (rb * 4 + w) * 1024 + (cb >> 1) * 256 + (cb & 1) * 8, sv);
    TS(7);
  }
  u32 wo3;
  {
    u32 row = lane >> 4, s = lane & 15;
    wo3 = (w * 4 + row) * 3584 + (((s >> 1) ^ (4 * (w & 1) + row)) << 5) + ((s & 1) << 4);
  }
  const char* w3b = w3 + ((fb * 224) & ~63u);
  WAITL0();
#pragma unroll
  for (int ks = 0; ks < 4; ++ks) {
    dma16<0>(wo3, w3b + ks * 114688, lds_w3(ks) + w * 1024);
    dma16<0>(wo3, w3b + ks * 114688 + 57344, lds_w3(ks) + 4096 + w * 1024);
  }
  WAITV(8);
  st2_sc1<1, 0>(zv, ws + WS_F2 + ((rb * 4 + w) * 8 + cb) * 8, tagv);
  TS(8);
  BAR();
#pragma unroll
  for (int ks = 4; ks < 16; ++ks) {
    dma16<0>(wo3, w3b + ks * 114688, lds_w3(ks) + w * 1024);
    dma16<0>(wo3, w3b + ks * 114688 + 57344, lds_w3(ks) + 4096 + w * 1024);
  }
  u32 a3[7];
#pragma unroll
  for (int t = 0; t < 7; ++t) a3[t] = (4 * g + q) * 256 + (((t + (fb & 1)) ^ (4 * (g & 1) + q)) << 5) + pp * 8;
  WAITV(0);
  BAR();
  {
    u32 ac0 = (4 * g + q) * 256 + (((w + (fb & 1)) ^ (4 * (g & 1) + q)) << 5) + pp * 8;
    u32 t1 = w < 3 ? w + 4 : 6;
    u32 ac1 = (4 * g + q) * 256 + (((t1 + (fb & 1)) ^ (4 * (g & 1) + q)) << 5) + pp * 8;
    f32x4 c0 = zero4, c1 = zero4;
#pragma unroll
    for (int ks = 0; ks < 16; ++ks) {
      c0 = mfma(cat(trd(ac0 + lds_w3(ks)), trd(ac0 + lds_w3(ks) + 4096)), ones, c0);
      c1 = mfma(cat(trd(ac1 + lds_w3(ks)), trd(ac1 + lds_w3(ks) + 4096)), ones, c1);
    }
    *(AS3 f32x4*)(L_MISC + 1024 + (w * 16 + g * 4) * 4) = c0;
    *(AS3 f32x4*)(L_MISC + 1024 + (t1 * 16 + g * 4) * 4) = c1;
    WAITL0();
    BAR();
  }
  TS(9);
  const u32 rb3 = rb128 * 2 + (w >> 1), tt3 = (w & 1) * 2;
  poll((lane & 15) * 8, ws + WS_F2 + (rb3 * 4 + tt3) * 64, tlo, thi);
  TS(10);
  {
    const char* sb = ws + WS_ST + (rb3 * 4 + tt3) * 1024;
    u32x4 st0 = ld4_sc1<0>(lin, sb), st1 = ld4_sc1<1024>(lin, sb);
    u32x4 xf[2][16];
    const char* xb = ws + WS_X1 + (rb3 * 4 + tt3) * 16384;
#define LDX(i) \
    xf[0][4 * i] = ld4_sc1<0>(lin, xb + i * 4096);        xf[1][4 * i] = ld4_sc1<0>(lin, xb + 16384 + i * 4096); \
    xf[0][4 * i + 1] = ld4_sc1<1024>(lin, xb + i * 4096); xf[1][4 * i + 1] = ld4_sc1<1024>(lin, xb + 16384 + i * 4096); \
    xf[0][4 * i + 2] = ld4_sc1<2048>(lin, xb + i * 4096); xf[1][4 * i + 2] = ld4_sc1<2048>(lin, xb + 16384 + i * 4096); \
    xf[0][4 * i + 3] = ld4_sc1<3072>(lin, xb + i * 4096); xf[1][4 * i + 3] = ld4_sc1<3072>(lin, xb + 16384 + i * 4096);
    LDX(0) LDX(1) LDX(2) LDX(3)
    f32x4 acc[2][7];
#pragma unroll
    for (int t = 0; t < 7; ++t) acc[0][t] = acc[1][t] = zero4;
#pragma unroll
    for (int ks = 0; ks < 16; ++ks) {
      asm volatile("s_waitcnt vmcnt(%2)" : "+v"(xf[0][ks]), "+v"(xf[1][ks]) : "i"(30 - 2 * ks) : "memory");
#pragma unroll
      for (int t = 0; t < 4; ++t) {
        bf16x8 af = cat(trd(a3[t] + lds_w3(ks)), trd(a3[t] + lds_w3(ks) + 4096));
        acc[0][t] = mfma(af, asb(xf[0][ks]), acc[0][t]);
        acc[1][t] = mfma(af, asb(xf[1][ks]), acc[1][t]);
      }
    }
    TS(11);
    PIN(st0); PIN(st1);
    float mean[2], c1[2], c2[2], sd[2];
#pragma unroll
    for (int i = 0; i < 2; ++i) {
      u32x4 s = i ? st1 : st0;
      float a = asf(s.x) + asf(s.z);
      float c = asf(s.y) + asf(s.w);
      a += __shfl_xor(a, 16); c += __shfl_xor(c, 16);
      a += __shfl_xor(a, 32); c += __shfl_xor(c, 32);
      mean[i] = a * (1.f / 512.f);
      float var = __builtin_fmaxf(c * (1.f / 512.f) - mean[i] * mean[i], 0.f) + 1e-5f;
      float rstd = __builtin_amdgcn_rsqf(var);
      sd[i] = var * rstd;
      c1[i] = GK1 * rstd;
      c2[i] = GK2 * rstd * rstd * rstd;
    }
    char* hb = ws + WS_H3 + ((rb128 * 16 + fb) * 4 + w) * 7168;
    u32 o[2][14];
#define EPI3(t) { f32x4 cs = *(AS3 f32x4*)(L_MISC + 1024 + (t * 16 + g * 4) * 4); \
    _Pragma("unroll") for (int i = 0; i < 2; ++i) { float v[4]; \
      _Pragma("unroll") for (int r = 0; r < 4; ++r) v[r] = gelu_ln(__builtin_fmaf(-mean[i], cs[r], acc[i][t][r]), c1[i], c2[i], sd[i]); \
      o[i][2 * t] = pk(v[0], v[1]); o[i][2 * t + 1] = pk(v[2], v[3]); } }
    EPI3(0) EPI3(1)
    st4_sc1<0>(lin, hb, (u32x4){o[0][0], o[0][1], o[0][2], o[0][3]});
    st4_sc1<1024>(lin, hb, (u32x4){o[1][0], o[1][1], o[1][2], o[1][3]});
    EPI3(2) EPI3(3)
    st4_sc1<2048>(lin, hb, (u32x4){o[0][4], o[0][5], o[0][6], o[0][7]});
    st4_sc1<3072>(lin, hb, (u32x4){o[1][4], o[1][5], o[1][6], o[1][7]});
    char* f3 = ws + WS_F3 + ((((rb128 * 2 + (w >> 1)) * 16 + fb) * 2 + (w & 1)) * 2) * 8;
#pragma unroll
    for (int ks = 0; ks < 16; ++ks) {
      if (ks == 8) {
        WAITV(0);
        st2_sc1<1, 0>(zv, f3, tagv);
      }
#pragma unroll
      for (int t = 4; t < 7; ++t) {
        bf16x8 af = cat(trd(a3[t] + lds_w3(ks)), trd(a3[t] + lds_w3(ks) + 4096));
        acc[0][t] = mfma(af, asb(xf[0][ks]), acc[0][t]);
        acc[1][t] = mfma(af, asb(xf[1][ks]), acc[1][t]);
      }
    }
    TS(12);
    EPI3(4) EPI3(5) EPI3(6)
    st4_sc1<0>(lin, hb + 4096, (u32x4){o[0][8], o[0][9], o[0][10], o[0][11]});
    st4_sc1<1024>(lin, hb + 4096, (u32x4){o[1][8], o[1][9], o[1][10], o[1][11]});
    st4_sc1<2048>(lin, hb + 4096, (u32x4){o[0][12], o[0][13], o[1][12], o[1][13]});
    BAR();
#pragma unroll
    for (int m = 11; m < 15; ++m) {
      dma16<0>(wo2, w4b + m * 16384, (m - 11) * 8192 + w * 1024);
      dma16<0>(wo2, w4b + m * 16384 + 8192, (m - 11) * 8192 + 4096 + w * 1024);
    }
    WAITV(8);
    st2_sc1<1, 0>(zv, f3 + 8, tagv);
    TS(13);
#pragma unroll
    for (int m = 15; m < 28; ++m) {
      dma16<0>(wo2, w4b + m * 16384, (m - 11) * 8192 + w * 1024);
      dma16<0>(wo2, w4b + m * 16384 + 8192, (m - 11) * 8192 + 4096 + w * 1024);
    }
  }
  poll((lane & 15) * 8, ws + WS_F3 + (((rb128 * 2 + (rb & 1)) * 16 + w * 4) * 4) * 8, tlo, thi);
  TS(14);
  {
#pragma unroll
    for (int m = 11; m < 28; ++m)
#pragma unroll
      for (int t = 0; t < 4; ++t) wf[m][t] = trd(a4[t] + (m - 11) * 8192);
    f32x4 acc[4][4];
#pragma unroll
    for (int i = 0; i < 4; ++i)
#pragma unroll
      for (int t = 0; t < 4; ++t) acc[i][t] = zero4;
    const char* hb = ws + WS_H3 + ((rb128 * 16 + w * 4) * 4 + (rb & 1) * 2) * 7168;
    u32x4 cu[2][2];
#pragma unroll
    for (int p = 0; p < 4; ++p) {
      u32x4 hf[2][7];
      const char* pb = hb + p * 28672;
#pragma unroll
      for (int v = 0; v < 2; ++v) {
        hf[v][0] = ld4_sc1<0>(lin, pb + v * 7168); hf[v][1] = ld4_sc1<1024>(lin, pb + v * 7168);
        hf[v][2] = ld4_sc1<2048>(lin, pb + v * 7168); hf[v][3] = ld4_sc1<3072>(lin, pb + v * 7168);
        hf[v][4] = ld4_sc1<0>(lin, pb + v * 7168 + 4096); hf[v][5] = ld4_sc1<1024>(lin, pb + v * 7168 + 4096);
        hf[v][6] = ld4_sc1<2048>(lin, pb + v * 7168 + 4096);
      }
      WAITV(0);
#pragma unroll
      for (int v = 0; v < 2; ++v)
#pragma unroll
        for (int u = 0; u < 7; ++u) PIN(hf[v][u]);
#pragma unroll
      for (int pr = 0; pr < 3; ++pr)
#pragma unroll
        for (int t = 0; t < 4; ++t) {
          bf16x8 af = cat(wf[p * 7 + 2 * pr][t], wf[p * 7 + 2 * pr + 1][t]);
#pragma unroll
          for (int i = 0; i < 4; ++i) acc[i][t] = mfma(af, asb(hf[i >> 1][2 * pr + (i & 1)]), acc[i][t]);
        }
      if ((p & 1) == 0) {
        cu[0][0] = hf[0][6]; cu[1][0] = hf[1][6];
      } else {
        cu[0][1] = hf[0][6]; cu[1][1] = hf[1][6];
#pragma unroll
        for (int t = 0; t < 4; ++t) {
          bf16x8 af = cat(wf[(p - 1) * 7 + 6][t], wf[p * 7 + 6][t]);
#pragma unroll
          for (int i = 0; i < 4; ++i) {
            u32x4 A = cu[i >> 1][0], B = cu[i >> 1][1];
            u32x4 f = (i & 1) ? (u32x4){A.z, A.w, B.z, B.w} : (u32x4){A.x, A.y, B.x, B.y};
            acc[i][t] = mfma(af, asb(f), acc[i][t]);
          }
        }
      }
    }
    BAR();
#pragma unroll
    for (int i = 0; i < 4; ++i) {
      if (i != (int)w) {
        u32 sl = (u32)i * 3 + (w > (u32)i ? w - 1 : w);
#pragma unroll
        for (int t = 0; t < 4; ++t) *(AS3 f32x4*)(sl * 4096 + t * 1024 + lin) = acc[i][t];
      }
    }
    WAITL0();
    BAR();
    f32x4 r[4];
#pragma unroll
    for (int t = 0; t < 4; ++t) {
      r[t] = w == 0 ? acc[0][t] : w == 1 ? acc[1][t] : w == 2 ? acc[2][t] : acc[3][t];
#pragma unroll
      for (int s = 0; s < 3; ++s) r[t] += *(AS3 f32x4*)((w * 3 + s) * 4096 + t * 1024 + lin);
    }
    u32 oo = (rb * 64 + w * 16 + t16) * 1024 + cb * 128 + g * 8;
#pragma unroll
    for (int t = 0; t < 4; ++t) {
      u32x2 d = {pk(r[t][0] + blo(x1p[2 * t]), r[t][1] + bhi(x1p[2 * t])),
                 pk(r[t][2] + blo(x1p[2 * t + 1]), r[t][3] + bhi(x1p[2 * t + 1]))};
      if (t == 0) st2<0>(oo, out, d);
      if (t == 1) st2<32>(oo, out, d);
      if (t == 2) st2<64>(oo, out, d);
      if (t == 3) st2<96>(oo, out, d);
    }
  }
  if (fb == 0 && w == 0) st2_sc1<1, 0>(zv, ws + WS_EP + rb128 * 128, tagv);
  {
    u64 tend = __builtin_amdgcn_s_memrealtime();
    while (__builtin_amdgcn_s_memrealtime() - t00 < 5000ull) __builtin_amdgcn_s_sleep(1);
    u64 mn = ~0ull, mx = 0;
    for (int i = 0; i < 4; ++i) {
      u32x4 r = ld4_sc1<0>((lane + 64 * i) * 16, ws + (20u << 20));
      asm volatile("s_waitcnt vmcnt(0)" : "+v"(r)::"memory");
      u64 tt = ((u64)r.y << 32) | r.x;
      bool okk = r.z == 0x1234567u && r.w == 0x89abcdu && (tt - t00 + 3000ull) < 6000ull;
      if (okk) { mn = tt < mn ? tt : mn; mx = tt > mx ? tt : mx; }
    }
    for (int o = 1; o < 64; o <<= 1) { u64 a = __shfl_xor(mn, o), c = __shfl_xor(mx, o); mn = a < mn ? a : mn; mx = c > mx ? c : mx; }
    u64 dl = mn + 10000ull;
    while ((long long)(__builtin_amdgcn_s_memrealtime() - dl) < 0) __builtin_amdgcn_s_sleep(1);
  }
}

extern "C" void kernel_launch(void* const* d_in, const int* in_sizes, int n_in, void* d_out, int out_size,
                              void* d_ws, size_t ws_size, hipStream_t stream) {
  (void)hipFuncSetAttribute((const void*)mixer, hipFuncAttributeMaxDynamicSharedMemorySize, L_TOTAL);
  mixer<<<256, 256, L_TOTAL, stream>>>((const char*)d_in[0], (const char*)d_in[1], (const char*)d_in[2],
                                       (const char*)d_in[3], (const char*)d_in[4], (char*)d_out, (char*)d_ws);
}
```

```cpp
#include <hip/hip_runtime.h>
#include <stdint.h>

typedef unsigned int u32;
typedef unsigned long long u64;
typedef u32 u32x4 __attribute__((ext_vector_type(4)));
typedef u32 u32x2 __attribute__((ext_vector_type(2)));
typedef float f32x4 __attribute__((ext_vector_type(4)));
typedef float f32x2 __attribute__((ext_vector_type(2)));
typedef short s16x4 __attribute__((ext_vector_type(4)));
typedef short s16x8 __attribute__((ext_vector_type(8)));
typedef __bf16 bf16x8 __attribute__((ext_vector_type(8)));
typedef __bf16 bf16x2 __attribute__((ext_vector_type(2)));
#define AS3 __attribute__((address_space(3)))
#define DEV __device__ __forceinline__

#define WS_HS    0u
#define WS_X1    (2u << 20)
#define WS_H3    (4u << 20)
#define WS_ST    (11u << 20)
#define WS_F1    (12u << 20)
#define WS_F2    (WS_F1 + 8192u)
#define WS_F3    (WS_F1 + 16384u)
#define WS_EP    (WS_F1 + 32768u)
#define WS_DBG   (16u << 20)

#define L_X    0u
#define L_W1   65536u
#define L_W2A  131072u
#define L_MISC 159744u
#define L_TOTAL 163840u
DEV constexpr u32 lds_w2(int ks) { return ks < 7 ? L_W2A + ks * 4096u : (ks - 7) * 4096u; }
DEV constexpr u32 lds_w3(int ks) { return ks < 11 ? 40960u + ks * 8192u : (ks - 11) * 8192u; }

#define WAITV(n) asm volatile("s_waitcnt vmcnt(%0)" ::"i"(n) : "memory")
#define WAITL0() asm volatile("s_waitcnt lgkmcnt(0)" ::: "memory")
#define BAR() do { asm volatile("" ::: "memory"); __builtin_amdgcn_s_barrier(); asm volatile("" ::: "memory"); } while (0)
#define PIN(x) asm volatile("" : "+v"(x))

template <int IMM>
DEV void dma16(u32 voff, const char* sbase, u32 lds) {
  asm volatile("s_mov_b32 m0, %2\n\ts_nop 0\n\tglobal_load_lds_dwordx4 %0, %1 offset:%3"
               ::"v"(voff), "s"(sbase), "s"(lds), "i"(IMM) : "memory");
}
template <int IMM>
DEV u32x4 ld4_sc1(u32 voff, const char* sbase) {
  u32x4 d;
  asm volatile("global_load_dwordx4 %0, %1, %2 offset:%3 sc1" : "=v"(d) : "v"(voff), "s"(sbase), "i"(IMM) : "memory");
  return d;
}
template <int IMM>
DEV u32x2 ld2(u32 voff, const char* sbase) {
  u32x2 d;
  asm volatile("global_load_dwordx2 %0, %1, %2 offset:%3" : "=v"(d) : "v"(voff), "s"(sbase), "i"(IMM) : "memory");
  return d;
}
DEV u32x2 ld2_sc1(u32 voff, const char* sbase) {
  u32x2 d;
  asm volatile("global_load_dwordx2 %0, %1, %2 sc1" : "=v"(d) : "v"(voff), "s"(sbase) : "memory");
  return d;
}
template <int IMM>
DEV void st4_sc1(u32 voff, char* sbase, u32x4 d) {
  asm volatile("global_store_dwordx4 %0, %1, %2 offset:%3 sc1\n\ts_nop 1" ::"v"(voff), "v"(d), "s"(sbase), "i"(IMM) : "memory");
}
template <u32 MLO, u32 MHI>
DEV void st2_sc1(u32 voff, char* sbase, u32x2 d) {
  asm volatile("s_mov_b32 exec_lo, %3\n\ts_mov_b32 exec_hi, %4\n\tglobal_store_dwordx2 %0, %1, %2 sc1\n\ts_mov_b64 exec, -1"
               ::"v"(voff), "v"(d), "s"(sbase), "i"(MLO), "i"(MHI) : "memory");
}
template <int IMM>
DEV void st2(u32 voff, char* sbase, u32x2 d) {
  asm volatile("global_store_dwordx2 %0, %1, %2 offset:%3" ::"v"(voff), "v"(d), "s"(sbase), "i"(IMM) : "memory");
}
DEV s16x4 trd(u32 a) { return __builtin_amdgcn_ds_read_tr16_b64_v4i16((AS3 s16x4*)a); }
DEV bf16x8 cat(s16x4 lo, s16x4 hi) {
  s16x8 v = __builtin_shufflevector(lo, hi, 0, 1, 2, 3, 4, 5, 6, 7);
  return __builtin_bit_cast(bf16x8, v);
}
DEV bf16x8 asb(u32x4 v) { return __builtin_bit_cast(bf16x8, v); }
DEV f32x4 mfma(bf16x8 a, bf16x8 b, f32x4 c) { return __builtin_amdgcn_mfma_f32_16x16x32_bf16(a, b, c, 0, 0, 0); }
DEV u32 pk(float a, float b) {
  f32x2 v = {a, b};
  return __builtin_bit_cast(u32, __builtin_convertvector(v, bf16x2));
}
DEV float asf(u32 v) { return __builtin_bit_cast(float, v); }
DEV float blo(u32 v) { return __builtin_bit_cast(float, v << 16); }
DEV float bhi(u32 v) { return __builtin_bit_cast(float, v & 0xffff0000u); }

DEV float gelu_ln(float t, float c1, float c2, float s) {
  float z = t * __builtin_fmaf(t * t, c2, c1);
  float e = __builtin_amdgcn_exp2f(z);
  return t * __builtin_amdgcn_rcpf(__builtin_fmaf(e, s, s));
}
#define GK1 (-2.302208198f)
#define GK2 (-0.1029432397f)

DEV void poll(u32 voff, const char* base, u32 tlo, u32 thi) {
  for (int it = 0; it < (1 << 16); ++it) {
    u32x2 f = ld2_sc1(voff, base);
    asm volatile("s_waitcnt vmcnt(0)" : "+v"(f)::"memory");
    if (__builtin_amdgcn_ballot_w64((f.x != tlo) | (f.y != thi)) == 0) break;
  }
}

#define TS(i)

__global__ __launch_bounds__(256, 1) void mixer(const char* __restrict__ x, const char* __restrict__ w1,
                                                const char* __restrict__ w2, const char* __restrict__ w3,
                                                const char* __restrict__ w4, char* __restrict__ out,
                                                char* __restrict__ ws) {
  const u64 t00 = __builtin_amdgcn_s_memrealtime();
  const u32 tid = threadIdx.x, lane = tid & 63;
  const u32 w = __builtin_amdgcn_readfirstlane(tid >> 6);
  const u32 b = blockIdx.x, xcd = b & 7, j = b >> 3;
  const u32 rb = xcd * 4 + (j >> 3), cb = j & 7;
  const u32 rb128 = xcd * 2 + (j >> 4), fb = j & 15;
  const u32 t16 = lane & 15, g = lane >> 4, q = (lane >> 2) & 3, pp = lane & 3;
  const u32 lin = lane * 16, zv = 0;
  const u64 epoch = *(const u64*)(ws + WS_EP + rb128 * 128);

  const u32 grp = cb >> 1;
  const int dlt = (grp & 1) ? (int)(1 + (grp >> 1)) : -(int)(1 + (grp >> 1));
  {
    int r = (int)(rb * 64 + w * 16 + (lane >> 2)) + dlt;
    r = r < 0 ? 0 : (r > 2047 ? 2047 : r);
    u32 xo = (u32)r * 1024 + ((pp ^ ((lane & 32) ? 3u : 0u)) << 4);
    u32 lx = L_X + w * 16384;
    u32 row = lane >> 3, s = lane & 7;
    u32 sw1 = ((row >> 1) & 1) | ((w & 1) << 1);
    u32 wo1 = (w * 8 + row) * 1024 + ((((s >> 1) ^ sw1) << 1 | (s & 1)) << 4);
    const char* w1b = w1 + cb * 128;
    u32 lw = L_W1 + w * 1024;
#define S1LD(ks) dma16<(ks) * 64>(xo, x, lx + (ks) * 960); dma16<0>(wo1, w1b + (ks) * 32768, lw + (ks) * 4096);
    S1LD(0) S1LD(1) S1LD(2) S1LD(3) S1LD(4) S1LD(5) S1LD(6) S1LD(7)
    S1LD(8) S1LD(9) S1LD(10) S1LD(11) S1LD(12) S1LD(13) S1LD(14) S1LD(15)
  }
  u32 wo2;
  {
    u32 row = lane >> 3, s = lane & 7;
    wo2 = row * 1024 + ((((s >> 1) ^ ((row >> 1) & 3)) << 1 | (s & 1)) << 4);
  }
  const char* w2b = w2 + cb * 128 + w * 8192;
#pragma unroll
  for (int ks = 0; ks < 7; ++ks) dma16<0>(wo2, w2b + ks * 32768, lds_w2(ks) + w * 1024);
  u32x2 xr[4];
  {
    u32 o = (rb * 64 + w * 16 + t16) * 1024 + cb * 128 + g * 8;
    xr[0] = ld2<0>(o, x); xr[1] = ld2<32>(o, x); xr[2] = ld2<64>(o, x); xr[3] = ld2<96>(o, x);
  }
  const u64 tag = epoch * 0x9E3779B97F4A7C15ull + 0xD1B54A32D192ED03ull;
  const u32 tlo = (u32)tag, thi = (u32)(tag >> 32);
  const u32x2 tagv = {tlo, thi};
  TS(1);

  const u32 cx = (q >> 1) | ((g & 1) << 1);
  const bf16x8 ones = asb((u32x4){0x3f803f80u, 0x3f803f80u, 0x3f803f80u, 0x3f803f80u});
  const f32x4 zero4 = {0.f, 0.f, 0.f, 0.f};

  {
    f32x4 acc[4] = {zero4, zero4, zero4, zero4}, asum = zero4, asq = zero4, acs = zero4;
    u32 xa = L_X + w * 16384 + (t16 * 4 + (g ^ ((t16 & 8) ? 3u : 0u))) * 16;
    u32 a1[4];
#pragma unroll
    for (int t = 0; t < 4; ++t) a1[t] = L_W1 + (8 * g + q) * 128 + ((t ^ cx) << 5) + pp * 8;
    u32 aw = L_W1 + (8 * g + q) * 128 + ((w ^ cx) << 5) + pp * 8;
#pragma unroll
    for (int k2 = 0; k2 < 8; ++k2) {
      WAITV(43 - 4 * (k2 + 1));
      BAR();
#pragma unroll
      for (int kk = 0; kk < 2; ++kk) {
        const int ks = k2 * 2 + kk;
        bf16x8 xf = asb(*(AS3 u32x4*)(xa + ks * 1024));
#pragma unroll
        for (int t = 0; t < 4; ++t) {
          bf16x8 af = cat(trd(a1[t] + ks * 4096), trd(a1[t] + ks * 4096 + 512));
          acc[t] = mfma(af, xf, acc[t]);
        }
        bf16x8 af = cat(trd(aw + ks * 4096), trd(aw + ks * 4096 + 512));
        acs = mfma(af, ones, acs);
        asum = mfma(ones, xf, asum);
        asq = mfma(xf, xf, asq);
      }
    }
    TS(2);
    *(AS3 f32x4*)(L_MISC + (w * 16 + g * 4) * 4) = acs;
    WAITL0();
    BAR();
#pragma unroll
    for (int ks = 7; ks < 16; ++ks) dma16<0>(wo2, w2b + ks * 32768, lds_w2(ks) + w * 1024);
    float sq = (pp == 0) ? asq[0] : (pp == 1) ? asq[1] : (pp == 2) ? asq[2] : asq[3];
    sq = __shfl(sq, (int)(t16 + 16 * q));
    float mean = asum[0] * (1.f / 512.f);
    float var = __builtin_fmaxf(sq * (1.f / 512.f) - mean * mean, 0.f) + 1e-5f;
    float rstd = __builtin_amdgcn_rsqf(var);
    float sd = var * rstd;
    float c1 = GK1 * rstd, c2 = GK2 * rstd * rstd * rstd;
    int sr = (int)(rb * 64 + w * 16 + t16) + dlt;
    bool ok = sr >= 0 && sr < 2048;
    u32 o[8];
#pragma unroll
    for (int t = 0; t < 4; ++t) {
      f32x4 cs = *(AS3 f32x4*)(L_MISC + (t * 16 + g * 4) * 4);
      float v[4];
#pragma unroll
      for (int r = 0; r < 4; ++r) v[r] = gelu_ln(__builtin_fmaf(-mean, cs[r], acc[t][r]), c1, c2, sd);
      o[2 * t] = ok ? pk(v[0], v[1]) : 0u;
      o[2 * t + 1] = ok ? pk(v[2], v[3]) : 0u;
    }
    char* hb = ws + WS_HS + ((rb * 4 + w) * 16 + cb * 2) * 1024;
    st4_sc1<0>(lin, hb, (u32x4){o[0], o[1], o[2], o[3]});
    st4_sc1<1024>(lin, hb, (u32x4){o[4], o[5], o[6], o[7]});
    TS(3);
  }
  const char* w4b = w4 + cb * 128 + w * (448 * 1024);
#pragma unroll
  for (int m = 0; m < 4; ++m) {
    dma16<0>(wo2, w4b + m * 16384, lds_w3(m) + w * 1024);
    dma16<0>(wo2, w4b + m * 16384 + 8192, lds_w3(m) + 4096 + w * 1024);
  }
  WAITV(8);
  st2_sc1<1, 0>(zv, ws + WS_F1 + ((rb * 4 + w) * 8 + cb) * 8, tagv);
  TS(4);
#pragma unroll
  for (int m = 4; m < 11; ++m) {
    dma16<0>(wo2, w4b + m * 16384, lds_w3(m) + w * 1024);
    dma16<0>(wo2, w4b + m * 16384 + 8192, lds_w3(m) + 4096 + w * 1024);
  }
  s16x4 wf[28][4];
  u32 a4[4];
#pragma unroll
  for (int t = 0; t < 4; ++t) a4[t] = w * 1024 + (g >> 1) * 4096 + (4 * (g & 1) + q) * 128 + ((t ^ cx) << 5) + pp * 8;
  WAITV(15);
#pragma unroll
  for (int m = 0; m < 4; ++m)
#pragma unroll
    for (int t = 0; t < 4; ++t) wf[m][t] = trd(a4[t] + lds_w3(m));

  poll((lane & 7) * 8, ws + WS_F1 + (rb * 4 + w) * 64, tlo, thi);
  TS(5);
  u32 x1p[8];
  {
    u32x4 hf[16];
    const char* hb = ws + WS_HS + (rb * 4 + w) * 16384;
#define LDH(i) hf[4 * i] = ld4_sc1<0>(lin, hb + i * 4096); hf[4 * i + 1] = ld4_sc1<1024>(lin, hb + i * 4096); \
               hf[4 * i + 2] = ld4_sc1<2048>(lin, hb + i * 4096); hf[4 * i + 3] = ld4_sc1<3072>(lin, hb + i * 4096);
    LDH(0) LDH(1) LDH(2) LDH(3)
#pragma unroll
    for (int m = 4; m < 11; ++m)
#pragma unroll
      for (int t = 0; t < 4; ++t) wf[m][t] = trd(a4[t] + lds_w3(m));
    BAR();
    u32 a2[4];
#pragma unroll
    for (int t = 0; t < 4; ++t) a2[t] = (4 * g + q) * 128 + ((t ^ cx) << 5) + pp * 8;
    f32x4 acc[4] = {zero4, zero4, zero4, zero4};
#pragma unroll
    for (int ks = 0; ks < 16; ++ks) {
      asm volatile("s_waitcnt vmcnt(%1)" : "+v"(hf[ks]) : "i"(15 - ks) : "memory");
#pragma unroll
      for (int t = 0; t < 4; ++t) {
        bf16x8 af = cat(trd(a2[t] + lds_w2(ks)), trd(a2[t] + lds_w2(ks) + 2048));
        acc[t] = mfma(af, asb(hf[ks]), acc[t]);
      }
    }
    TS(6);
#pragma unroll
    for (int t = 0; t < 4; ++t) {
      PIN(xr[t]);
      x1p[2 * t] = pk(acc[t][0] + blo(xr[t].x), acc[t][1] + bhi(xr[t].x));
      x1p[2 * t + 1] = pk(acc[t][2] + blo(xr[t].y), acc[t][3] + bhi(xr[t].y));
    }
    u32x4 f0 = {x1p[0], x1p[1], x1p[2], x1p[3]}, f1 = {x1p[4], x1p[5], x1p[6], x1p[7]};
    char* xb = ws + WS_X1 + ((rb * 4 + w) * 16 + cb * 2) * 1024;
    st4_sc1<0>(lin, xb, f0);
    st4_sc1<1024>(lin, xb, f1);
    f32x4 s1 = mfma(ones, asb(f0), zero4), s2 = mfma(asb(f0), asb(f0), zero4);
    s1 = mfma(ones, asb(f1), s1);
    s2 = mfma(asb(f1), asb(f1), s2);
    float sq = (pp == 0) ? s2[0] : (pp == 1) ? s2[1] : (pp == 2) ? s2[2] : s2[3];
    u32x2 sv = {__builtin_bit_cast(u32, s1[0]), __builtin_bit_cast(u32, sq)};
    st2_sc1<0x00F0000Fu, 0xF0000F00u>(t16 * 16, ws + WS_ST + (rb * 4 + w) * 1024 + (cb >> 1) * 256 + (cb & 1) * 8, sv);
    TS(7);
  }
  u32 wo3;
  {
    u32 row = lane >> 4, s = lane & 15;
    wo3 = (w * 4 + row) * 3584 + (((s >> 1) ^ (4 * (w & 1) + row)) << 5) + ((s & 1) << 4);
  }
  const char* w3b = w3 + ((fb * 224) & ~63u);
  WAITL0();
#pragma unroll
  for (int ks = 0; ks < 4; ++ks) {
    dma16<0>(wo3, w3b + ks * 114688, lds_w3(ks) + w * 1024);
    dma16<0>(wo3, w3b + ks * 114688 + 57344, lds_w3(ks) + 4096 + w * 1024);
  }
  WAITV(8);
  st2_sc1<1, 0>(zv, ws + WS_F2 + ((rb * 4 + w) * 8 + cb) * 8, tagv);
  TS(8);
  BAR();
#pragma unroll
  for (int ks = 4; ks < 16; ++ks) {
    dma16<0>(wo3, w3b + ks * 114688, lds_w3(ks) + w * 1024);
    dma16<0>(wo3, w3b + ks * 114688 + 57344, lds_w3(ks) + 4096 + w * 1024);
  }
  u32 a3[7];
#pragma unroll
  for (int t = 0; t < 7; ++t) a3[t] = (4 * g + q) * 256 + (((t + (fb & 1)) ^ (4 * (g & 1) + q)) << 5) + pp * 8;
  WAITV(0);
  BAR();
  {
    u32 ac0 = (4 * g + q) * 256 + (((w + (fb & 1)) ^ (4 * (g & 1) + q)) << 5) + pp * 8;
    u32 t1 = w < 3 ? w + 4 : 6;
    u32 ac1 = (4 * g + q) * 256 + (((t1 + (fb & 1)) ^ (4 * (g & 1) + q)) << 5) + pp * 8;
    f32x4 c0 = zero4, c1 = zero4;
#pragma unroll
    for (int ks = 0; ks < 16; ++ks) {
      c0 = mfma(cat(trd(ac0 + lds_w3(ks)), trd(ac0 + lds_w3(ks) + 4096)), ones, c0);
      c1 = mfma(cat(trd(ac1 + lds_w3(ks)), trd(ac1 + lds_w3(ks) + 4096)), ones, c1);
    }
    *(AS3 f32x4*)(L_MISC + 1024 + (w * 16 + g * 4) * 4) = c0;
    *(AS3 f32x4*)(L_MISC + 1024 + (t1 * 16 + g * 4) * 4) = c1;
    WAITL0();
    BAR();
  }
  TS(9);
  const u32 rb3 = rb128 * 2 + (w >> 1), tt3 = (w & 1) * 2;
  poll((lane & 15) * 8, ws + WS_F2 + (rb3 * 4 + tt3) * 64, tlo, thi);
  TS(10);
  {
    const char* sb = ws + WS_ST + (rb3 * 4 + tt3) * 1024;
    u32x4 st0 = ld4_sc1<0>(lin, sb), st1 = ld4_sc1<1024>(lin, sb);
    u32x4 xf[2][16];
    const char* xb = ws + WS_X1 + (rb3 * 4 + tt3) * 16384;
#define LDX(i) \
    xf[0][4 * i] = ld4_sc1<0>(lin, xb + i * 4096);        xf[1][4 * i] = ld4_sc1<0>(lin, xb + 16384 + i * 4096); \
    xf[0][4 * i + 1] = ld4_sc1<1024>(lin, xb + i * 4096); xf[1][4 * i + 1] = ld4_sc1<1024>(lin, xb + 16384 + i * 4096); \
    xf[0][4 * i + 2] = ld4_sc1<2048>(lin, xb + i * 4096); xf[1][4 * i + 2] = ld4_sc1<2048>(lin, xb + 16384 + i * 4096); \
    xf[0][4 * i + 3] = ld4_sc1<3072>(lin, xb + i * 4096); xf[1][4 * i + 3] = ld4_sc1<3072>(lin, xb + 16384 + i * 4096);
    LDX(0) LDX(1) LDX(2) LDX(3)
    f32x4 acc[2][7];
#pragma unroll
    for (int t = 0; t < 7; ++t) acc[0][t] = acc[1][t] = zero4;
#pragma unroll
    for (int ks = 0; ks < 16; ++ks) {
      asm volatile("s_waitcnt vmcnt(%2)" : "+v"(xf[0][ks]), "+v"(xf[1][ks]) : "i"(30 - 2 * ks) : "memory");
#pragma unroll
      for (int t = 0; t < 4; ++t) {
        bf16x8 af = cat(trd(a3[t] + lds_w3(ks)), trd(a3[t] + lds_w3(ks) + 4096));
        acc[0][t] = mfma(af, asb(xf[0][ks]), acc[0][t]);
        acc[1][t] = mfma(af, asb(xf[1][ks]), acc[1][t]);
      }
    }
    TS(11);
    PIN(st0); PIN(st1);
    float mean[2], c1[2], c2[2], sd[2];
#pragma unroll
    for (int i = 0; i < 2; ++i) {
      u32x4 s = i ? st1 : st0;
      float a = asf(s.x) + asf(s.z);
      float c = asf(s.y) + asf(s.w);
      a += __shfl_xor(a, 16); c += __shfl_xor(c, 16);
      a += __shfl_xor(a, 32); c += __shfl_xor(c, 32);
      mean[i] = a * (1.f / 512.f);
      float var = __builtin_fmaxf(c * (1.f / 512.f) - mean[i] * mean[i], 0.f) + 1e-5f;
      float rstd = __builtin_amdgcn_rsqf(var);
      sd[i] = var * rstd;
      c1[i] = GK1 * rstd;
      c2[i] = GK2 * rstd * rstd * rstd;
    }
    char* hb = ws + WS_H3 + ((rb128 * 16 + fb) * 4 + w) * 7168;
    u32 o[2][14];
#define EPI3(t) { f32x4 cs = *(AS3 f32x4*)(L_MISC + 1024 + (t * 16 + g * 4) * 4); \
    _Pragma("unroll") for (int i = 0; i < 2; ++i) { float v[4]; \
      _Pragma("unroll") for (int r = 0; r < 4; ++r) v[r] = gelu_ln(__builtin_fmaf(-mean[i], cs[r], acc[i][t][r]), c1[i], c2[i], sd[i]); \
      o[i][2 * t] = pk(v[0], v[1]); o[i][2 * t + 1] = pk(v[2], v[3]); } }
    EPI3(0) EPI3(1)
    st4_sc1<0>(lin, hb, (u32x4){o[0][0], o[0][1], o[0][2], o[0][3]});
    st4_sc1<1024>(lin, hb, (u32x4){o[1][0], o[1][1], o[1][2], o[1][3]});
    EPI3(2) EPI3(3)
    st4_sc1<2048>(lin, hb, (u32x4){o[0][4], o[0][5], o[0][6], o[0][7]});
    st4_sc1<3072>(lin, hb, (u32x4){o[1][4], o[1][5], o[1][6], o[1][7]});
    char* f3 = ws + WS_F3 + ((((rb128 * 2 + (w >> 1)) * 16 + fb) * 2 + (w & 1)) * 2) * 8;
#pragma unroll
    for (int ks = 0; ks < 16; ++ks) {
      if (ks == 8) {
        WAITV(0);
        st2_sc1<1, 0>(zv, f3, tagv);
      }
#pragma unroll
      for (int t = 4; t < 7; ++t) {
        bf16x8 af = cat(trd(a3[t] + lds_w3(ks)), trd(a3[t] + lds_w3(ks) + 4096));
        acc[0][t] = mfma(af, asb(xf[0][ks]), acc[0][t]);
        acc[1][t] = mfma(af, asb(xf[1][ks]), acc[1][t]);
      }
    }
    TS(12);
    EPI3(4) EPI3(5) EPI3(6)
    st4_sc1<0>(lin, hb + 4096, (u32x4){o[0][8], o[0][9], o[0][10], o[0][11]});
    st4_sc1<1024>(lin, hb + 4096, (u32x4){o[1][8], o[1][9], o[1][10], o[1][11]});
    st4_sc1<2048>(lin, hb + 4096, (u32x4){o[0][12], o[0][13], o[1][12], o[1][13]});
    BAR();
#pragma unroll
    for (int m = 11; m < 15; ++m) {
      dma16<0>(wo2, w4b + m * 16384, (m - 11) * 8192 + w * 1024);
      dma16<0>(wo2, w4b + m * 16384 + 8192, (m - 11) * 8192 + 4096 + w * 1024);
    }
    WAITV(8);
    st2_sc1<1, 0>(zv, f3 + 8, tagv);
    TS(13);
#pragma unroll
    for (int m = 15; m < 28; ++m) {
      dma16<0>(wo2, w4b + m * 16384, (m - 11) * 8192 + w * 1024);
      dma16<0>(wo2, w4b + m * 16384 + 8192, (m - 11) * 8192 + 4096 + w * 1024);
    }
  }
  poll((lane & 15) * 8, ws + WS_F3 + (((rb128 * 2 + (rb & 1)) * 16 + w * 4) * 4) * 8, tlo, thi);
  TS(14);
  {
#pragma unroll
    for (int m = 11; m < 28; ++m)
#pragma unroll
      for (int t = 0; t < 4; ++t) wf[m][t] = trd(a4[t] + (m - 11) * 8192);
    f32x4 acc[4][4];
#pragma unroll
    for (int i = 0; i < 4; ++i)
#pragma unroll
      for (int t = 0; t < 4; ++t) acc[i][t] = zero4;
    const char* hb = ws + WS_H3 + ((rb128 * 16 + w * 4) * 4 + (rb & 1) * 2) * 7168;
    u32x4 cu[2][2];
#pragma unroll
    for (int p = 0; p < 4; ++p) {
      u32x4 hf[2][7];
      const char* pb = hb + p * 28672;
#pragma unroll
      for (int v = 0; v < 2; ++v) {
        hf[v][0] = ld4_sc1<0>(lin, pb + v * 7168); hf[v][1] = ld4_sc1<1024>(lin, pb + v * 7168);
        hf[v][2] = ld4_sc1<2048>(lin, pb + v * 7168); hf[v][3] = ld4_sc1<3072>(lin, pb + v * 7168);
        hf[v][4] = ld4_sc1<0>(lin, pb + v * 7168 + 4096); hf[v][5] = ld4_sc1<1024>(lin, pb + v * 7168 + 4096);
        hf[v][6] = ld4_sc1<2048>(lin, pb + v * 7168 + 4096);
      }
      WAITV(0);
#pragma unroll
      for (int v = 0; v < 2; ++v)
#pragma unroll
        for (int u = 0; u < 7; ++u) PIN(hf[v][u]);
#pragma unroll
      for (int pr = 0; pr < 3; ++pr)
#pragma unroll
        for (int t = 0; t < 4; ++t) {
          bf16x8 af = cat(wf[p * 7 + 2 * pr][t], wf[p * 7 + 2 * pr + 1][t]);
#pragma unroll
          for (int i = 0; i < 4; ++i) acc[i][t] = mfma(af, asb(hf[i >> 1][2 * pr + (i & 1)]), acc[i][t]);
        }
      if ((p & 1) == 0) {
        cu[0][0] = hf[0][6]; cu[1][0] = hf[1][6];
      } else {
        cu[0][1] = hf[0][6]; cu[1][1] = hf[1][6];
#pragma unroll
        for (int t = 0; t < 4; ++t) {
          bf16x8 af = cat(wf[(p - 1) * 7 + 6][t], wf[p * 7 + 6][t]);
#pragma unroll
          for (int i = 0; i < 4; ++i) {
            u32x4 A = cu[i >> 1][0], B = cu[i >> 1][1];
            u32x4 f = (i & 1) ? (u32x4){A.z, A.w, B.z, B.w} : (u32x4){A.x, A.y, B.x, B.y};
            acc[i][t] = mfma(af, asb(f), acc[i][t]);
          }
        }
      }
    }
    BAR();
#pragma unroll
    for (int i = 0; i < 4; ++i) {
      if (i != (int)w) {
        u32 sl = (u32)i * 3 + (w > (u32)i ? w - 1 : w);
#pragma unroll
        for (int t = 0; t < 4; ++t) *(AS3 f32x4*)(sl * 4096 + t * 1024 + lin) = acc[i][t];
      }
    }
    WAITL0();
    BAR();
    f32x4 r[4];
#pragma unroll
    for (int t = 0; t < 4; ++t) {
      r[t] = w == 0 ? acc[0][t] : w == 1 ? acc[1][t] : w == 2 ? acc[2][t] : acc[3][t];
#pragma unroll
      for (int s = 0; s < 3; ++s) r[t] += *(AS3 f32x4*)((w * 3 + s) * 4096 + t * 1024 + lin);
    }
    u32 oo = (rb * 64 + w * 16 + t16) * 1024 + cb * 128 + g * 8;
#pragma unroll
    for (int t = 0; t < 4; ++t) {
      u32x2 d = {pk(r[t][0] + blo(x1p[2 * t]), r[t][1] + bhi(x1p[2 * t])),
                 pk(r[t][2] + blo(x1p[2 * t + 1]), r[t][3] + bhi(x1p[2 * t + 1]))};
      if (t == 0) st2<0>(oo, out, d);
      if (t == 1) st2<32>(oo, out, d);
      if (t == 2) st2<64>(oo, out, d);
      if (t == 3) st2<96>(oo, out, d);
    }
  }
  if (fb == 0 && w == 0) st2_sc1<1, 0>(zv, ws + WS_EP + rb128 * 128, tagv);
  while (__builtin_amdgcn_s_memrealtime() - t00 < 10000ull) __builtin_amdgcn_s_sleep(1);
}

extern "C" void kernel_launch(void* const* d_in, const int* in_sizes, int n_in, void* d_out, int out_size,
                              void* d_ws, size_t ws_size, hipStream_t stream) {
  (void)hipFuncSetAttribute((const void*)mixer, hipFuncAttributeMaxDynamicSharedMemorySize, L_TOTAL);
  mixer<<<256, 256, L_TOTAL, stream>>>((const char*)d_in[0], (const char*)d_in[1], (const char*)d_in[2],
                                       (const char*)d_in[3], (const char*)d_in[4], (char*)d_out, (char*)d_ws);
}
```

```cpp
#include <hip/hip_runtime.h>
#include <stdint.h>

typedef unsigned int u32;
typedef unsigned long long u64;
typedef u32 u32x4 __attribute__((ext_vector_type(4)));
typedef u32 u32x2 __attribute__((ext_vector_type(2)));
typedef float f32x4 __attribute__((ext_vector_type(4)));
typedef float f32x2 __attribute__((ext_vector_type(2)));
typedef short s16x4 __attribute__((ext_vector_type(4)));
typedef short s16x8 __attribute__((ext_vector_type(8)));
typedef __bf16 bf16x8 __attribute__((ext_vector_type(8)));
typedef __bf16 bf16x2 __attribute__((ext_vector_type(2)));
#define AS3 __attribute__((address_space(3)))
#define DEV __device__ __forceinline__

#define WS_HS    0u
#define WS_X1    (2u << 20)
#define WS_H3    (4u << 20)
#define WS_ST    (11u << 20)
#define WS_F1    (12u << 20)
#define WS_F2    (WS_F1 + 8192u)
#define WS_F3    (WS_F1 + 16384u)
#define WS_EP    (WS_F1 + 32768u)
#define WS_DBG   (16u << 20)

#define L_X    0u
#define L_W1   65536u
#define L_W2A  131072u
#define L_MISC 159744u
#define L_TOTAL 163840u
DEV constexpr u32 lds_w2(int ks) { return ks < 7 ? L_W2A + ks * 4096u : (ks - 7) * 4096u; }
DEV constexpr u32 lds_w3(int ks) { return ks < 11 ? 40960u + ks * 8192u : (ks - 11) * 8192u; }

#define WAITV(n) asm volatile("s_waitcnt vmcnt(%0)" ::"i"(n) : "memory")
#define WAITL0() asm volatile("s_waitcnt lgkmcnt(0)" ::: "memory")
#define BAR() do { asm volatile("" ::: "memory"); __builtin_amdgcn_s_barrier(); asm volatile("" ::: "memory"); } while (0)
#define PIN(x) asm volatile("" : "+v"(x))

template <int IMM>
DEV void dma16(u32 voff, const char* sbase, u32 lds) {
  asm volatile("s_mov_b32 m0, %2\n\ts_nop 0\n\tglobal_load_lds_dwordx4 %0, %1 offset:%3"
               ::"v"(voff), "s"(sbase), "s"(lds), "i"(IMM) : "memory");
}
template <int IMM>
DEV u32x4 ld4_sc1(u32 voff, const char* sbase) {
  u32x4 d;
  asm volatile("global_load_dwordx4 %0, %1, %2 offset:%3 sc1" : "=v"(d) : "v"(voff), "s"(sbase), "i"(IMM) : "memory");
  return d;
}
template <int IMM>
DEV u32x2 ld2(u32 voff, const char* sbase) {
  u32x2 d;
  asm volatile("global_load_dwordx2 %0, %1, %2 offset:%3" : "=v"(d) : "v"(voff), "s"(sbase), "i"(IMM) : "memory");
  return d;
}
DEV u32x2 ld2_sc1(u32 voff, const char* sbase) {
  u32x2 d;
  asm volatile("global_load_dwordx2 %0, %1, %2 sc1" : "=v"(d) : "v"(voff), "s"(sbase) : "memory");
  return d;
}
template <int IMM>
DEV void st4_sc1(u32 voff, char* sbase, u32x4 d) {
  asm volatile("global_store_dwordx4 %0, %1, %2 offset:%3 sc1\n\ts_nop 1" ::"v"(voff), "v"(d), "s"(sbase), "i"(IMM) : "memory");
}
template <u32 MLO, u32 MHI>
DEV void st2_sc1(u32 voff, char* sbase, u32x2 d) {
  asm volatile("s_mov_b32 exec_lo, %3\n\ts_mov_b32 exec_hi, %4\n\tglobal_store_dwordx2 %0, %1, %2 sc1\n\ts_mov_b64 exec, -1"
               ::"v"(voff), "v"(d), "s"(sbase), "i"(MLO), "i"(MHI) : "memory");
}
template <int IMM>
DEV void st2(u32 voff, char* sbase, u32x2 d) {
  asm volatile("global_store_dwordx2 %0, %1, %2 offset:%3" ::"v"(voff), "v"(d), "s"(sbase), "i"(IMM) : "memory");
}
DEV s16x4 trd(u32 a) { return __builtin_amdgcn_ds_read_tr16_b64_v4i16((AS3 s16x4*)a); }
DEV bf16x8 cat(s16x4 lo, s16x4 hi) {
  s16x8 v = __builtin_shufflevector(lo, hi, 0, 1, 2, 3, 4, 5, 6, 7);
  return __builtin_bit_cast(bf16x8, v);
}
DEV bf16x8 asb(u32x4 v) { return __builtin_bit_cast(bf16x8, v); }
DEV f32x4 mfma(bf16x8 a, bf16x8 b, f32x4 c) { return __builtin_amdgcn_mfma_f32_16x16x32_bf16(a, b, c, 0, 0, 0); }
DEV u32 pk(float a, float b) {
  f32x2 v = {a, b};
  return __builtin_bit_cast(u32, __builtin_convertvector(v, bf16x2));
}
DEV float asf(u32 v) { return __builtin_bit_cast(float, v); }
DEV float blo(u32 v) { return __builtin_bit_cast(float, v << 16); }
DEV float bhi(u32 v) { return __builtin_bit_cast(float, v & 0xffff0000u); }

DEV float gelu_ln(float t, float c1, float c2, float s) {
  float z = t * __builtin_fmaf(t * t, c2, c1);
  float e = __builtin_amdgcn_exp2f(z);
  return t * __builtin_amdgcn_rcpf(__builtin_fmaf(e, s, s));
}
#define GK1 (-2.302208198f)
#define GK2 (-0.1029432397f)

DEV void poll(u32 voff, const char* base, u32 tlo, u32 thi) {
  for (int it = 0; it < (1 << 16); ++it) {
    u32x2 f = ld2_sc1(voff, base);
    asm volatile("s_waitcnt vmcnt(0)" : "+v"(f)::"memory");
    if (__builtin_amdgcn_ballot_w64((f.x != tlo) | (f.y != thi)) == 0) break;
  }
}

#define TS(i)

__global__ __launch_bounds__(256, 1) void mixer(const char* __restrict__ x, const char* __restrict__ w1,
                                                const char* __restrict__ w2, const char* __restrict__ w3,
                                                const char* __restrict__ w4, char* __restrict__ out,
                                                char* __restrict__ ws) {
  u64 t00;
  asm volatile("s_memrealtime %0" : "=s"(t00));
  __builtin_amdgcn_sched_barrier(0);
  const u32 tid = threadIdx.x, lane = tid & 63;
  const u32 w = __builtin_amdgcn_readfirstlane(tid >> 6);
  const u32 b = blockIdx.x, xcd = b & 7, j = b >> 3;
  const u32 rb = xcd * 4 + (j >> 3), cb = j & 7;
  const u32 rb128 = xcd * 2 + (j >> 4), fb = j & 15;
  const u32 t16 = lane & 15, g = lane >> 4, q = (lane >> 2) & 3, pp = lane & 3;
  const u32 lin = lane * 16, zv = 0;
  const u64 epoch = *(const u64*)(ws + WS_EP + rb128 * 128);

  const u32 grp = cb >> 1;
  const int dlt = (grp & 1) ? (int)(1 + (grp >> 1)) : -(int)(1 + (grp >> 1));
  {
    int r = (int)(rb * 64 + w * 16 + (lane >> 2)) + dlt;
    r = r < 0 ? 0 : (r > 2047 ? 2047 : r);
    u32 xo = (u32)r * 1024 + ((pp ^ ((lane & 32) ? 3u : 0u)) << 4);
    u32 lx = L_X + w * 16384;
    u32 row = lane >> 3, s = lane & 7;
    u32 sw1 = ((row >> 1) & 1) | ((w & 1) << 1);
    u32 wo1 = (w * 8 + row) * 1024 + ((((s >> 1) ^ sw1) << 1 | (s & 1)) << 4);
    const char* w1b = w1 + cb * 128;
    u32 lw = L_W1 + w * 1024;
#define S1LD(ks) dma16<(ks) * 64>(xo, x, lx + (ks) * 960); dma16<0>(wo1, w1b + (ks) * 32768, lw + (ks) * 4096);
    S1LD(0) S1LD(1) S1LD(2) S1LD(3) S1LD(4) S1LD(5) S1LD(6) S1LD(7)
    S1LD(8) S1LD(9) S1LD(10) S1LD(11) S1LD(12) S1LD(13) S1LD(14) S1LD(15)
  }
  u32 wo2;
  {
    u32 row = lane >> 3, s = lane & 7;
    wo2 = row * 1024 + ((((s >> 1) ^ ((row >> 1) & 3)) << 1 | (s & 1)) << 4);
  }
  const char* w2b = w2 + cb * 128 + w * 8192;
#pragma unroll
  for (int ks = 0; ks < 7; ++ks) dma16<0>(wo2, w2b + ks * 32768, lds_w2(ks) + w * 1024);
  u32x2 xr[4];
  {
    u32 o = (rb * 64 + w * 16 + t16) * 1024 + cb * 128 + g * 8;
    xr[0] = ld2<0>(o, x); xr[1] = ld2<32>(o, x); xr[2] = ld2<64>(o, x); xr[3] = ld2<96>(o, x);
  }
  const u64 tag = epoch * 0x9E3779B97F4A7C15ull + 0xD1B54A32D192ED03ull;
  const u32 tlo = (u32)tag, thi = (u32)(tag >> 32);
  const u32x2 tagv = {tlo, thi};
  asm volatile("s_waitcnt lgkmcnt(0)" : "+s"(t00));
  TS(1);

  const u32 cx = (q >> 1) | ((g & 1) << 1);
  const bf16x8 ones = asb((u32x4){0x3f803f80u, 0x3f803f80u, 0x3f803f80u, 0x3f803f80u});
  const f32x4 zero4 = {0.f, 0.f, 0.f, 0.f};

  {
    f32x4 acc[4] = {zero4, zero4, zero4, zero4}, asum = zero4, asq = zero4, acs = zero4;
    u32 xa = L_X + w * 16384 + (t16 * 4 + (g ^ ((t16 & 8) ? 3u : 0u))) * 16;
    u32 a1[4];
#pragma unroll
    for (int t = 0; t < 4; ++t) a1[t] = L_W1 + (8 * g + q) * 128 + ((t ^ cx) << 5) + pp * 8;
    u32 aw = L_W1 + (8 * g + q) * 128 + ((w ^ cx) << 5) + pp * 8;
#pragma unroll
    for (int k2 = 0; k2 < 8; ++k2) {
      WAITV(43 - 4 * (k2 + 1));
      BAR();
#pragma unroll
      for (int kk = 0; kk < 2; ++kk) {
        const int ks = k2 * 2 + kk;
        bf16x8 xf = asb(*(AS3 u32x4*)(xa + ks * 1024));
#pragma unroll
        for (int t = 0; t < 4; ++t) {
          bf16x8 af = cat(trd(a1[t] + ks * 4096), trd(a1[t] + ks * 4096 + 512));
          acc[t] = mfma(af, xf, acc[t]);
        }
        bf16x8 af = cat(trd(aw + ks * 4096), trd(aw + ks * 4096 + 512));
        acs = mfma(af, ones, acs);
        asum = mfma(ones, xf, asum);
        asq = mfma(xf, xf, asq);
      }
    }
    TS(2);
    *(AS3 f32x4*)(L_MISC + (w * 16 + g * 4) * 4) = acs;
    WAITL0();
    BAR();
#pragma unroll
    for (int ks = 7; ks < 16; ++ks) dma16<0>(wo2, w2b + ks * 32768, lds_w2(ks) + w * 1024);
    float sq = (pp == 0) ? asq[0] : (pp == 1) ? asq[1] : (pp == 2) ? asq[2] : asq[3];
    sq = __shfl(sq, (int)(t16 + 16 * q));
    float mean = asum[0] * (1.f / 512.f);
    float var = __builtin_fmaxf(sq * (1.f / 512.f) - mean * mean, 0.f) + 1e-5f;
    float rstd = __builtin_amdgcn_rsqf(var);
    float sd = var * rstd;
    float c1 = GK1 * rstd, c2 = GK2 * rstd * rstd * rstd;
    int sr = (int)(rb * 64 + w * 16 + t16) + dlt;
    bool ok = sr >= 0 && sr < 2048;
    u32 o[8];
#pragma unroll
    for (int t = 0; t < 4; ++t) {
      f32x4 cs = *(AS3 f32x4*)(L_MISC + (t * 16 + g * 4) * 4);
      float v[4];
#pragma unroll
      for (int r = 0; r < 4; ++r) v[r] = gelu_ln(__builtin_fmaf(-mean, cs[r], acc[t][r]), c1, c2, sd);
      o[2 * t] = ok ? pk(v[0], v[1]) : 0u;
      o[2 * t + 1] = ok ? pk(v[2], v[3]) : 0u;
    }
    char* hb = ws + WS_HS + ((rb * 4 + w) * 16 + cb * 2) * 1024;
    st4_sc1<0>(lin, hb, (u32x4){o[0], o[1], o[2], o[3]});
    st4_sc1<1024>(lin, hb, (u32x4){o[4], o[5], o[6], o[7]});
    TS(3);
  }
  const char* w4b = w4 + cb * 128 + w * (448 * 1024);
#pragma unroll
  for (int m = 0; m < 4; ++m) {
    dma16<0>(wo2, w4b + m * 16384, lds_w3(m) + w * 1024);
    dma16<0>(wo2, w4b + m * 16384 + 8192, lds_w3(m) + 4096 + w * 1024);
  }
  WAITV(8);
  st2_sc1<1, 0>(zv, ws + WS_F1 + ((rb * 4 + w) * 8 + cb) * 8, tagv);
  TS(4);
#pragma unroll
  for (int m = 4; m < 11; ++m) {
    dma16<0>(wo2, w4b + m * 16384, lds_w3(m) + w * 1024);
    dma16<0>(wo2, w4b + m * 16384 + 8192, lds_w3(m) + 4096 + w * 1024);
  }
  s16x4 wf[28][4];
  u32 a4[4];
#pragma unroll
  for (int t = 0; t < 4; ++t) a4[t] = w * 1024 + (g >> 1) * 4096 + (4 * (g & 1) + q) * 128 + ((t ^ cx) << 5) + pp * 8;
  WAITV(15);
#pragma unroll
  for (int m = 0; m < 4; ++m)
#pragma unroll
    for (int t = 0; t < 4; ++t) wf[m][t] = trd(a4[t] + lds_w3(m));

  poll((lane & 7) * 8, ws + WS_F1 + (rb * 4 + w) * 64, tlo, thi);
  TS(5);
  u32 x1p[8];
  {
    u32x4 hf[16];
    const char* hb = ws + WS_HS + (rb * 4 + w) * 16384;
#define LDH(i) hf[4 * i] = ld4_sc1<0>(lin, hb + i * 4096); hf[4 * i + 1] = ld4_sc1<1024>(lin, hb + i * 4096); \
               hf[4 * i + 2] = ld4_sc1<2048>(lin, hb + i * 4096); hf[4 * i + 3] = ld4_sc1<3072>(lin, hb + i * 4096);
    LDH(0) LDH(1) LDH(2) LDH(3)
#pragma unroll
    for (int m = 4; m < 11; ++m)
#pragma unroll
      for (int t = 0; t < 4; ++t) wf[m][t] = trd(a4[t] + lds_w3(m));
    BAR();
    u32 a2[4];
#pragma unroll
    for (int t = 0; t < 4; ++t) a2[t] = (4 * g + q) * 128 + ((t ^ cx) << 5) + pp * 8;
    f32x4 acc[4] = {zero4, zero4, zero4, zero4};
#pragma unroll
    for (int ks = 0; ks < 16; ++ks) {
      asm volatile("s_waitcnt vmcnt(%1)" : "+v"(hf[ks]) : "i"(15 - ks) : "memory");
#pragma unroll
      for (int t = 0; t < 4; ++t) {
        bf16x8 af = cat(trd(a2[t] + lds_w2(ks)), trd(a2[t] + lds_w2(ks) + 2048));
        acc[t] = mfma(af, asb(hf[ks]), acc[t]);
      }
    }
    TS(6);
#pragma unroll
    for (int t = 0; t < 4; ++t) {
      PIN(xr[t]);
      x1p[2 * t] = pk(acc[t][0] + blo(xr[t].x), acc[t][1] + bhi(xr[t].x));
      x1p[2 * t + 1] = pk(acc[t][2] + blo(xr[t].y), acc[t][3] + bhi(xr[t].y));
    }
    u32x4 f0 = {x1p[0], x1p[1], x1p[2], x1p[3]}, f1 = {x1p[4], x1p[5], x1p[6], x1p[7]};
    char* xb = ws + WS_X1 + ((rb * 4 + w) * 16 + cb * 2) * 1024;
    st4_sc1<0>(lin, xb, f0);
    st4_sc1<1024>(lin, xb, f1);
    f32x4 s1 = mfma(ones, asb(f0), zero4), s2 = mfma(asb(f0), asb(f0), zero4);
    s1 = mfma(ones, asb(f1), s1);
    s2 = mfma(asb(f1), asb(f1), s2);
    float sq = (pp == 0) ? s2[0] : (pp == 1) ? s2[1] : (pp == 2) ? s2[2] : s2[3];
    u32x2 sv = {__builtin_bit_cast(u32, s1[0]), __builtin_bit_cast(u32, sq)};
    st2_sc1<0x00F0000Fu, 0xF0000F00u>(t16 * 16, ws + WS_ST + (rb * 4 + w) * 1024 + (cb >> 1) * 256 + (cb & 1) * 8, sv);
    TS(7);
  }
  u32 wo3;
  {
    u32 row = lane >> 4, s = lane & 15;
    wo3 = (w * 4 + row) * 3584 + (((s >> 1) ^ (4 * (w & 1) + row)) << 5) + ((s & 1) << 4);
  }
  const char* w3b = w3 + ((fb * 224) & ~63u);
  WAITL0();
#pragma unroll
  for (int ks = 0; ks < 4; ++ks) {
    dma16<0>(wo3, w3b + ks * 114688, lds_w3(ks) + w * 1024);
    dma16<0>(wo3, w3b + ks * 114688 + 57344, lds_w3(ks) + 4096 + w * 1024);
  }
  WAITV(8);
  st2_sc1<1, 0>(zv, ws + WS_F2 + ((rb * 4 + w) * 8 + cb) * 8, tagv);
  TS(8);
  BAR();
#pragma unroll
  for (int ks = 4; ks < 16; ++ks) {
    dma16<0>(wo3, w3b + ks * 114688, lds_w3(ks) + w * 1024);
    dma16<0>(wo3, w3b + ks * 114688 + 57344, lds_w3(ks) + 4096 + w * 1024);
  }
  u32 a3[7];
#pragma unroll
  for (int t = 0; t < 7; ++t) a3[t] = (4 * g + q) * 256 + (((t + (fb & 1)) ^ (4 * (g & 1) + q)) << 5) + pp * 8;
  WAITV(0);
  BAR();
  {
    u32 ac0 = (4 * g + q) * 256 + (((w + (fb & 1)) ^ (4 * (g & 1) + q)) << 5) + pp * 8;
    u32 t1 = w < 3 ? w + 4 : 6;
    u32 ac1 = (4 * g + q) * 256 + (((t1 + (fb & 1)) ^ (4 * (g & 1) + q)) << 5) + pp * 8;
    f32x4 c0 = zero4, c1 = zero4;
#pragma unroll
    for (int ks = 0; ks < 16; ++ks) {
      c0 = mfma(cat(trd(ac0 + lds_w3(ks)), trd(ac0 + lds_w3(ks) + 4096)), ones, c0);
      c1 = mfma(cat(trd(ac1 + lds_w3(ks)), trd(ac1 + lds_w3(ks) + 4096)), ones, c1);
    }
    *(AS3 f32x4*)(L_MISC + 1024 + (w * 16 + g * 4) * 4) = c0;
    *(AS3 f32x4*)(L_MISC + 1024 + (t1 * 16 + g * 4) * 4) = c1;
    WAITL0();
    BAR();
  }
  TS(9);
  const u32 rb3 = rb128 * 2 + (w >> 1), tt3 = (w & 1) * 2;
  poll((lane & 15) * 8, ws + WS_F2 + (rb3 * 4 + tt3) * 64, tlo, thi);
  TS(10);
  {
    const char* sb = ws + WS_ST + (rb3 * 4 + tt3) * 1024;
    u32x4 st0 = ld4_sc1<0>(lin, sb), st1 = ld4_sc1<1024>(lin, sb);
    u32x4 xf[2][16];
    const char* xb = ws + WS_X1 + (rb3 * 4 + tt3) * 16384;
#define LDX(i) \
    xf[0][4 * i] = ld4_sc1<0>(lin, xb + i * 4096);        xf[1][4 * i] = ld4_sc1<0>(lin, xb + 16384 + i * 4096); \
    xf[0][4 * i + 1] = ld4_sc1<1024>(lin, xb + i * 4096); xf[1][4 * i + 1] = ld4_sc1<1024>(lin, xb + 16384 + i * 4096); \
    xf[0][4 * i + 2] = ld4_sc1<2048>(lin, xb + i * 4096); xf[1][4 * i + 2] = ld4_sc1<2048>(lin, xb + 16384 + i * 4096); \
    xf[0][4 * i + 3] = ld4_sc1<3072>(lin, xb + i * 4096); xf[1][4 * i + 3] = ld4_sc1<3072>(lin, xb + 16384 + i * 4096);
    LDX(0) LDX(1) LDX(2) LDX(3)
    f32x4 acc[2][7];
#pragma unroll
    for (int t = 0; t < 7; ++t) acc[0][t] = acc[1][t] = zero4;
#pragma unroll
    for (int ks = 0; ks < 16; ++ks) {
      asm volatile("s_waitcnt vmcnt(%2)" : "+v"(xf[0][ks]), "+v"(xf[1][ks]) : "i"(30 - 2 * ks) : "memory");
#pragma unroll
      for (int t = 0; t < 4; ++t) {
        bf16x8 af = cat(trd(a3[t] + lds_w3(ks)), trd(a3[t] + lds_w3(ks) + 4096));
        acc[0][t] = mfma(af, asb(xf[0][ks]), acc[0][t]);
        acc[1][t] = mfma(af, asb(xf[1][ks]), acc[1][t]);
      }
    }
    TS(11);
    PIN(st0); PIN(st1);
    float mean[2], c1[2], c2[2], sd[2];
#pragma unroll
    for (int i = 0; i < 2; ++i) {
      u32x4 s = i ? st1 : st0;
      float a = asf(s.x) + asf(s.z);
      float c = asf(s.y) + asf(s.w);
      a += __shfl_xor(a, 16); c += __shfl_xor(c, 16);
      a += __shfl_xor(a, 32); c += __shfl_xor(c, 32);
      mean[i] = a * (1.f / 512.f);
      float var = __builtin_fmaxf(c * (1.f / 512.f) - mean[i] * mean[i], 0.f) + 1e-5f;
      float rstd = __builtin_amdgcn_rsqf(var);
      sd[i] = var * rstd;
      c1[i] = GK1 * rstd;
      c2[i] = GK2 * rstd * rstd * rstd;
    }
    char* hb = ws + WS_H3 + ((rb128 * 16 + fb) * 4 + w) * 7168;
    u32 o[2][14];
#define EPI3(t) { f32x4 cs = *(AS3 f32x4*)(L_MISC + 1024 + (t * 16 + g * 4) * 4); \
    _Pragma("unroll") for (int i = 0; i < 2; ++i) { float v[4]; \
      _Pragma("unroll") for (int r = 0; r < 4; ++r) v[r] = gelu_ln(__builtin_fmaf(-mean[i], cs[r], acc[i][t][r]), c1[i], c2[i], sd[i]); \
      o[i][2 * t] = pk(v[0], v[1]); o[i][2 * t + 1] = pk(v[2], v[3]); } }
    EPI3(0) EPI3(1)
    st4_sc1<0>(lin, hb, (u32x4){o[0][0], o[0][1], o[0][2], o[0][3]});
    st4_sc1<1024>(lin, hb, (u32x4){o[1][0], o[1][1], o[1][2], o[1][3]});
    EPI3(2) EPI3(3)
    st4_sc1<2048>(lin, hb, (u32x4){o[0][4], o[0][5], o[0][6], o[0][7]});
    st4_sc1<3072>(lin, hb, (u32x4){o[1][4], o[1][5], o[1][6], o[1][7]});
    char* f3 = ws + WS_F3 + ((((rb128 * 2 + (w >> 1)) * 16 + fb) * 2 + (w & 1)) * 2) * 8;
#pragma unroll
    for (int ks = 0; ks < 16; ++ks) {
      if (ks == 8) {
        WAITV(0);
        st2_sc1<1, 0>(zv, f3, tagv);
      }
#pragma unroll
      for (int t = 4; t < 7; ++t) {
        bf16x8 af = cat(trd(a3[t] + lds_w3(ks)), trd(a3[t] + lds_w3(ks) + 4096));
        acc[0][t] = mfma(af, asb(xf[0][ks]), acc[0][t]);
        acc[1][t] = mfma(af, asb(xf[1][ks]), acc[1][t]);
      }
    }
    TS(12);
    EPI3(4) EPI3(5) EPI3(6)
    st4_sc1<0>(lin, hb + 4096, (u32x4){o[0][8], o[0][9], o[0][10], o[0][11]});
    st4_sc1<1024>(lin, hb + 4096, (u32x4){o[1][8], o[1][9], o[1][10], o[1][11]});
    st4_sc1<2048>(lin, hb + 4096, (u32x4){o[0][12], o[0][13], o[1][12], o[1][13]});
    BAR();
#pragma unroll
    for (int m = 11; m < 15; ++m) {
      dma16<0>(wo2, w4b + m * 16384, (m - 11) * 8192 + w * 1024);
      dma16<0>(wo2, w4b + m * 16384 + 8192, (m - 11) * 8192 + 4096 + w * 1024);
    }
    WAITV(8);
    st2_sc1<1, 0>(zv, f3 + 8, tagv);
    TS(13);
#pragma unroll
    for (int m = 15; m < 28; ++m) {
      dma16<0>(wo2, w4b + m * 16384, (m - 11) * 8192 + w * 1024);
      dma16<0>(wo2, w4b + m * 16384 + 8192, (m - 11) * 8192 + 4096 + w * 1024);
    }
  }
  poll((lane & 15) * 8, ws + WS_F3 + (((rb128 * 2 + (rb & 1)) * 16 + w * 4) * 4) * 8, tlo, thi);
  TS(14);
  {
#pragma unroll
    for (int m = 11; m < 28; ++m)
#pragma unroll
      for (int t = 0; t < 4; ++t) wf[m][t] = trd(a4[t] + (m - 11) * 8192);
    f32x4 acc[4][4];
#pragma unroll
    for (int i = 0; i < 4; ++i)
#pragma unroll
      for (int t = 0; t < 4; ++t) acc[i][t] = zero4;
    const char* hb = ws + WS_H3 + ((rb128 * 16 + w * 4) * 4 + (rb & 1) * 2) * 7168;
    u32x4 cu[2][2];
#pragma unroll
    for (int p = 0; p < 4; ++p) {
      u32x4 hf[2][7];
      const char* pb = hb + p * 28672;
#pragma unroll
      for (int v = 0; v < 2; ++v) {
        hf[v][0] = ld4_sc1<0>(lin, pb + v * 7168); hf[v][1] = ld4_sc1<1024>(lin, pb + v * 7168);
        hf[v][2] = ld4_sc1<2048>(lin, pb + v * 7168); hf[v][3] = ld4_sc1<3072>(lin, pb + v * 7168);
        hf[v][4] = ld4_sc1<0>(lin, pb + v * 7168 + 4096); hf[v][5] = ld4_sc1<1024>(lin, pb + v * 7168 + 4096);
        hf[v][6] = ld4_sc1<2048>(lin, pb + v * 7168 + 4096);
      }
      WAITV(0);
#pragma unroll
      for (int v = 0; v < 2; ++v)
#pragma unroll
        for (int u = 0; u < 7; ++u) PIN(hf[v][u]);
#pragma unroll
      for (int pr = 0; pr < 3; ++pr)
#pragma unroll
        for (int t = 0; t < 4; ++t) {
          bf16x8 af = cat(wf[p * 7 + 2 * pr][t], wf[p * 7 + 2 * pr + 1][t]);
#pragma unroll
          for (int i = 0; i < 4; ++i) acc[i][t] = mfma(af, asb(hf[i >> 1][2 * pr + (i & 1)]), acc[i][t]);
        }
      if ((p & 1) == 0) {
        cu[0][0] = hf[0][6]; cu[1][0] = hf[1][6];
      } else {
        cu[0][1] = hf[0][6]; cu[1][1] = hf[1][6];
#pragma unroll
        for (int t = 0; t < 4; ++t) {
          bf16x8 af = cat(wf[(p - 1) * 7 + 6][t], wf[p * 7 + 6][t]);
#pragma unroll
          for (int i = 0; i < 4; ++i) {
            u32x4 A = cu[i >> 1][0], B = cu[i >> 1][1];
            u32x4 f = (i & 1) ? (u32x4){A.z, A.w, B.z, B.w} : (u32x4){A.x, A.y, B.x, B.y};
            acc[i][t] = mfma(af, asb(f), acc[i][t]);
          }
        }
      }
    }
    BAR();
#pragma unroll
    for (int i = 0; i < 4; ++i) {
      if (i != (int)w) {
        u32 sl = (u32)i * 3 + (w > (u32)i ? w - 1 : w);
#pragma unroll
        for (int t = 0; t < 4; ++t) *(AS3 f32x4*)(sl * 4096 + t * 1024 + lin) = acc[i][t];
      }
    }
    WAITL0();
    BAR();
    f32x4 r[4];
#pragma unroll
    for (int t = 0; t < 4; ++t) {
      r[t] = w == 0 ? acc[0][t] : w == 1 ? acc[1][t] : w == 2 ? acc[2][t] : acc[3][t];
#pragma unroll
      for (int s = 0; s < 3; ++s) r[t] += *(AS3 f32x4*)((w * 3 + s) * 4096 + t * 1024 + lin);
    }
    u32 oo = (rb * 64 + w * 16 + t16) * 1024 + cb * 128 + g * 8;
#pragma unroll
    for (int t = 0; t < 4; ++t) {
      u32x2 d = {pk(r[t][0] + blo(x1p[2 * t]), r[t][1] + bhi(x1p[2 * t])),
                 pk(r[t][2] + blo(x1p[2 * t + 1]), r[t][3] + bhi(x1p[2 * t + 1]))};
      if (t == 0) st2<0>(oo, out, d);
      if (t == 1) st2<32>(oo, out, d);
      if (t == 2) st2<64>(oo, out, d);
      if (t == 3) st2<96>(oo, out, d);
    }
  }
  if (fb == 0 && w == 0) st2_sc1<1, 0>(zv, ws + WS_EP + rb128 * 128, tagv);
  while (__builtin_amdgcn_s_memrealtime() - t00 < 5000ull) __builtin_amdgcn_s_sleep(1);
}

extern "C" void kernel_launch(void* const* d_in, const int* in_sizes, int n_in, void* d_out, int out_size,
                              void* d_ws, size_t ws_size, hipStream_t stream) {
  (void)hipFuncSetAttribute((const void*)mixer, hipFuncAttributeMaxDynamicSharedMemorySize, L_TOTAL);
  mixer<<<256, 256, L_TOTAL, stream>>>((const char*)d_in[0], (const char*)d_in[1], (const char*)d_in[2],
                                       (const char*)d_in[3], (const char*)d_in[4], (char*)d_out, (char*)d_ws);
}
```

```cpp
#include <hip/hip_runtime.h>
#include <stdint.h>
#define NSTAMP 0
#define WS_DBG (64u << 20)
#define G_STRIDE_LOG 21
#define G_HS 0
#define G_X1 131072
#define G_H3 262144
#define L_TOTAL 163840
__global__ __launch_bounds__(256, 1) void mixer(const void* x, const void* w1, const void* w2, const void* w3, const void* w4, void* out, void* ws) {
  asm volatile(
    "s_load_dwordx2 s[32:33], %0, 0\n"
    "s_load_dwordx2 s[36:37], %0, 8\n"
    "s_load_dwordx2 s[40:41], %0, 16\n"
    "s_load_dwordx2 s[44:45], %0, 24\n"
    "s_load_dwordx2 s[48:49], %0, 32\n"
    "s_load_dwordx2 s[52:53], %0, 40\n"
    "s_load_dwordx2 s[56:57], %0, 48\n"
    "s_getpc_b64 s[62:63]\n"
    "LPC_%=:\n"
    "v_lshlrev_b32 v110, 7, %2\n"
    "v_min_u32 v110, LEND_%=-LPC_%=-4, v110\n"
    "global_load_dword v109, v110, s[62:63]\n"
    "v_lshrrev_b32 v36, 6, %2\n"
    "v_and_b32 v48, 63, %2\n"
    "s_lshr_b32 s28, %1, 3\n"
    "v_readfirstlane_b32 s8, v36\n"
    "s_bfe_u32 s29, %1, 0x20001\n"
    "s_and_b32 s30, %1, 1\n"
    "s_lshr_b32 s31, s28, 2\n"
    "s_lshl3_add_u32 s66, s29, s31\n"
    "s_and_b32 s31, s28, 3\n"
    "s_lshl2_add_u32 s67, s30, s31\n"
    "s_and_b32 s31, s28, 7\n"
    "s_lshl3_add_u32 s68, s30, s31\n"
    "s_add_u32 s89, s66, s67\n"
    "s_sub_u32 s89, s66, s67\n"
    "s_lshr_b32 s31, s28, 3\n"
    "s_add_u32 s90, s28, s31\n"
    "s_add_u32 s72, s89, 0\n"
    "s_and_b32 s72, s72, 3\n"
    "s_lshl_b32 s72, s72, 17\n"
    "s_add_u32 s73, s89, 1\n"
    "s_and_b32 s73, s73, 3\n"
    "s_lshl_b32 s73, s73, 17\n"
    "s_add_u32 s74, s89, 2\n"
    "s_and_b32 s74, s74, 3\n"
    "s_lshl_b32 s74, s74, 17\n"
    "s_add_u32 s75, s89, 3\n"
    "s_and_b32 s75, s75, 3\n"
    "s_lshl_b32 s75, s75, 17\n"
    "s_lshr_b32 s28, s67, 2\n"
    "s_add_u32 s28, s28, 1\n"
    "s_sub_u32 s29, 0, s28\n"
    "s_bitcmp1_b32 s67, 1\n"
    "s_cselect_b32 s28, s28, s29\n"
    "s_lshl_b32 s29, s66, 6\n"
    "s_add_u32 s28, s28, s29\n"
    "s_lshl4_add_u32 s28, s8, s28\n"
    "s_lshl_b32 s9, s8, 10\n"
    "s_mov_b32 s34, 0x200000\n"
    "s_mov_b32 s35, 0x20000\n"
    "s_mov_b32 s38, 0x80000\n"
    "s_mov_b32 s39, 0x20000\n"
    "v_lshrrev_b32 v36, 2, v48\n"
    "v_add_u32 v36, s28, v36\n"
    "v_bfe_i32 v37, v48, 5, 1\n"
    "v_xor_b32 v37, v37, v48\n"
    "v_and_b32 v37, 3, v37\n"
    "v_lshlrev_b32 v37, 4, v37\n"
    "v_lshl_add_u32 v120, v36, 10, v37\n"
    "v_bfe_u32 v36, v48, 4, 1\n"
    "s_and_b32 s29, s8, 1\n"
    "s_lshl_b32 s29, s29, 1\n"
    "v_or_b32 v36, s29, v36\n"
    "v_lshlrev_b32 v36, 1, v36\n"
    "v_xor_b32 v36, v36, v48\n"
    "v_and_b32 v36, 7, v36\n"
    "v_lshrrev_b32 v37, 3, v48\n"
    "v_lshlrev_b32 v37, 10, v37\n"
    "v_lshl_add_u32 v121, v36, 4, v37\n"
    "s_lshl_b32 s29, s8, 13\n"
    "s_lshl_b32 s30, s67, 7\n"
    "s_add_u32 s11, s29, s30\n"
    "v_add_u32 v121, s11, v121\n"
    "s_waitcnt lgkmcnt(0)\n"
    "s_lshr_b32 s88, s72, 9\n"
    "s_add_u32 m0, s9, 0x1000\n"
    "s_add_u32 s31, s72, 0\n"
    "buffer_load_dwordx4 v120, s[32:35], s88 offen lds\n"
    "s_add_u32 m0, s9, 0\n"
    "s_nop 0\n"
    "buffer_load_dwordx4 v121, s[36:39], s31 offen lds\n"
    "s_add_u32 m0, s9, 0x2fc0\n"
    "s_add_u32 s31, s72, 0x8000\n"
    "buffer_load_dwordx4 v120, s[32:35], s88 offen offset:64 lds\n"
    "s_add_u32 m0, s9, 0x2000\n"
    "s_nop 0\n"
    "buffer_load_dwordx4 v121, s[36:39], s31 offen lds\n"
    "s_add_u32 m0, s9, 0x4f80\n"
    "s_add_u32 s31, s72, 0x10000\n"
    "buffer_load_dwordx4 v120, s[32:35], s88 offen offset:128 lds\n"
    "s_add_u32 m0, s9, 0x4000\n"
    "s_nop 0\n"
    "buffer_load_dwordx4 v121, s[36:39], s31 offen lds\n"
    "s_add_u32 m0, s9, 0x6f40\n"
    "s_add_u32 s31, s72, 0x18000\n"
    "buffer_load_dwordx4 v120, s[32:35], s88 offen offset:192 lds\n"
    "s_add_u32 m0, s9, 0x6000\n"
    "s_nop 0\n"
    "buffer_load_dwordx4 v121, s[36:39], s31 offen lds\n"
    "s_mov_b32 s54, 0x200000\n"
    "s_mov_b32 s55, 0x20000\n"
    "s_mov_b32 s58, 0x200000\n"
    "s_mov_b32 s59, 0x20000\n"
    "s_lshr_b32 s28, s66, 1\n"
    "s_lshl_b32 s28, s28, 21\n"
    "s_add_u32 s56, s56, s28\n"
    "s_addc_u32 s57, s57, 0\n"
    "v_mov_b32 v36, 0\n"
    "s_mov_b32 s28, 0xc0000\n"
    "buffer_load_dwordx2 v[26:27], v36, s[56:59], s28 offen sc1\n"
    "s_mov_b32 s42, 0x80000\n"
    "s_mov_b32 s43, 0x20000\n"
    "s_mov_b32 s46, 0x1c0000\n"
    "s_mov_b32 s47, 0x20000\n"
    "s_mov_b32 s50, 0x1c0000\n"
    "s_mov_b32 s51, 0x20000\n"
    "s_sub_u32 s44, s44, 0x1c000\n"
    "s_subb_u32 s45, s45, 0\n"
    "s_mov_b32 s46, 0x1dc000\n"
    "s_lshr_b32 s28, %1, 3\n"
    "s_bfe_u32 s29, %1, 0x20001\n"
    "s_lshl_b32 s29, s29, 5\n"
    "s_add_u32 s28, s28, s29\n"
    "s_mul_i32 s28, s28, 4\n"
    "s_mul_i32 s29, s8, 1\n"
    "s_add_u32 s28, s28, s29\n"
    "s_mul_i32 s28, s28, 0xe00\n"
    "s_and_b32 s29, %1, 1\n"
    "s_mul_i32 s29, s29, 0x700\n"
    "s_add_u32 s28, s28, s29\n"
    "s_add_u32 s28, s28, 0x1c000\n"
    "v_lshrrev_b32 v36, 4, v48\n"
    "v_and_b32 v37, 15, v48\n"
    "v_mul_u32_u24 v110, 0xe00, v36\n"
    "v_lshl_add_u32 v110, v37, 7, v110\n"
    "v_add_u32 v110, s28, v110\n"
    "v_mov_b32 v38, 0x80000000\n"
    "v_cmp_gt_u32 vcc, 1, v36\n"
    "s_nop 1\n"
    "v_cndmask_b32 v110, v38, v110, vcc\n"
    "v_cmp_gt_u32 vcc, 14, v37\n"
    "s_nop 1\n"
    "v_cndmask_b32 v110, v38, v110, vcc\n"
    "s_lshr_b32 s28, %1, 3\n"
    "s_bfe_u32 s29, %1, 0x20001\n"
    "s_lshl_b32 s29, s29, 5\n"
    "s_add_u32 s28, s28, s29\n"
    "s_mul_i32 s28, s28, 14\n"
    "s_mul_i32 s29, s8, 4\n"
    "s_add_u32 s28, s28, s29\n"
    "s_sub_u32 s30, 14, s29\n"
    "s_min_u32 s30, s30, 4\n"
    "s_mul_i32 s28, s28, 0x400\n"
    "s_and_b32 s29, %1, 1\n"
    "s_mul_i32 s29, s29, 0x200\n"
    "s_add_u32 s28, s28, s29\n"
    "v_lshrrev_b32 v36, 2, v48\n"
    "v_and_b32 v37, 3, v48\n"
    "v_mul_u32_u24 v111, 0x400, v36\n"
    "v_lshl_add_u32 v111, v37, 7, v111\n"
    "v_add_u32 v111, s28, v111\n"
    "v_mov_b32 v38, 0x80000000\n"
    "v_cmp_gt_u32 vcc, s30, v36\n"
    "s_nop 1\n"
    "v_cndmask_b32 v111, v38, v111, vcc\n"
    "s_lshl_b32 s10, s8, 11\n"
    "s_lshr_b32 s69, s8, 1\n"
    "s_and_b32 s70, s8, 1\n"
    "s_and_b32 s71, s66, 1\n"
    "s_mul_i32 s28, s8, 0x70000\n"
    "s_lshl_b32 s29, s67, 7\n"
    "s_add_u32 s12, s28, s29\n"
    "s_lshl2_add_u32 s28, s71, s8\n"
    "s_lshl_b32 s17, s28, 14\n"
    "s_lshl1_add_u32 s29, s67, s70\n"
    "s_lshl_b32 s29, s29, 10\n"
    "s_add_u32 s13, s17, s29\n"
    "s_xor_b32 s14, s13, 0x4000\n"
    "s_lshl_b32 s29, s67, 11\n"
    "s_add_u32 s18, s17, s29\n"
    "s_add_u32 s18, s18, 0x20000\n"
    "s_lshl3_add_u32 s29, s28, s67\n"
    "s_lshl_b32 s29, s29, 7\n"
    "s_add_u32 s19, s29, 0xb0000\n"
    "s_lshl1_add_u32 s28, s71, s69\n"
    "s_lshl_b32 s28, s28, 11\n"
    "s_add_u32 s16, s28, 0xb4000\n"
    "s_lshl1_add_u32 s29, s67, s70\n"
    "s_lshl_b32 s29, s29, 7\n"
    "s_add_u32 s15, s16, s29\n"
    "s_lshl3_add_u32 s29, s70, s67\n"
    "s_lshl_b32 s29, s29, 7\n"
    "s_add_u32 s20, s28, s29\n"
    "s_add_u32 s20, s20, 0xb6000\n"
    "s_lshl_b32 s28, s8, 11\n"
    "s_add_u32 s21, s28, 0xb6000\n"
    "s_lshl_b32 s28, s8, 15\n"
    "s_add_u32 s22, s28, 0x20000\n"
    "s_lshl_b32 s28, s8, 11\n"
    "s_add_u32 s23, s28, 0xb0000\n"
    "s_lshl2_add_u32 s28, s68, s8\n"
    "s_mul_i32 s28, s28, 0x1c00\n"
    "s_add_u32 s24, s28, 0x40000\n"
    "s_lshr_b32 s28, s68, 2\n"
    "s_lshl2_add_u32 s28, s69, s28\n"
    "s_lshl_b32 s28, s28, 5\n"
    "s_and_b32 s29, s68, 3\n"
    "s_lshl1_add_u32 s29, s29, s70\n"
    "s_add_u32 s28, s28, s29\n"
    "s_lshl_b32 s28, s28, 7\n"
    "s_add_u32 s25, s28, 0xb8000\n"
    "s_lshl2_add_u32 s28, s71, s8\n"
    "s_lshl_b32 s28, s28, 12\n"
    "s_add_u32 s26, s28, 0xb8000\n"
    "s_lshl3_add_u32 s28, s8, s71\n"
    "s_mul_i32 s28, s28, 0x3800\n"
    "s_add_u32 s27, s28, 0x40000\n"
    "s_or_b32 s61, s68, s8\n"
    "s_lshr_b32 s28, s72, 5\n"
    "s_mul_i32 s28, s28, 7\n"
    "s_add_u32 s80, s27, s28\n"
    "s_lshl2_add_u32 s76, s28, s12\n"
    "s_lshr_b32 s28, s73, 5\n"
    "s_mul_i32 s28, s28, 7\n"
    "s_add_u32 s81, s27, s28\n"
    "s_lshl2_add_u32 s77, s28, s12\n"
    "s_lshr_b32 s28, s74, 5\n"
    "s_mul_i32 s28, s28, 7\n"
    "s_add_u32 s82, s27, s28\n"
    "s_lshl2_add_u32 s78, s28, s12\n"
    "s_lshr_b32 s28, s75, 5\n"
    "s_mul_i32 s28, s28, 7\n"
    "s_add_u32 s83, s27, s28\n"
    "s_lshl2_add_u32 s79, s28, s12\n"
    "s_bfe_u32 s28, s89, 0x10001\n"
    "s_mul_i32 s28, s28, 0xe000\n"
    "s_add_u32 s86, s27, s28\n"
    "s_lshl2_add_u32 s84, s28, s12\n"
    "s_sub_u32 s28, 0xe000, s28\n"
    "s_add_u32 s87, s27, s28\n"
    "s_lshl2_add_u32 s85, s28, s12\n"
    "v_lshlrev_b32 v1, 4, v48\n"
    "v_and_b32 v49, 15, v48\n"
    "v_lshrrev_b32 v50, 4, v48\n"
    "v_bfe_u32 v51, v48, 2, 2\n"
    "v_and_b32 v52, 3, v48\n"
    "v_lshlrev_b32 v36, 1, v50\n"
    "v_xor_b32 v36, v36, v48\n"
    "v_and_b32 v36, 7, v36\n"
    "v_lshrrev_b32 v37, 3, v48\n"
    "v_lshlrev_b32 v37, 10, v37\n"
    "v_lshl_add_u32 v2, v36, 4, v37\n"
    "v_add_u32 v3, 0x1c00, v2\n"
    "v_add_u32 v127, s11, v2\n"
    "s_lshl2_add_u32 s28, s66, s8\n"
    "s_lshl_b32 s28, s28, 14\n"
    "s_lshl_b32 s29, s67, 7\n"
    "s_add_u32 s28, s28, s29\n"
    "v_lshlrev_b32 v36, 10, v49\n"
    "v_add_u32 v36, s28, v36\n"
    "v_and_b32 v37, 1, v50\n"
    "v_lshl_add_u32 v36, v37, 5, v36\n"
    "v_lshrrev_b32 v37, 1, v50\n"
    "v_lshl_add_u32 v22, v37, 4, v36\n"
    "v_lshrrev_b32 v36, 1, v51\n"
    "v_and_b32 v37, 1, v50\n"
    "v_lshl_or_b32 v53, v37, 1, v36\n"
    "v_lshrrev_b32 v38, 2, v48\n"
    "v_lshlrev_b32 v36, 7, v38\n"
    "v_lshl_add_u32 v36, v52, 3, v36\n"
    "v_xor_b32 v37, 0, v53\n"
    "v_lshl_add_u32 v4, v37, 5, v36\n"
    "v_xor_b32 v37, 1, v53\n"
    "v_lshl_add_u32 v5, v37, 5, v36\n"
    "v_xor_b32 v37, 2, v53\n"
    "v_lshl_add_u32 v6, v37, 5, v36\n"
    "v_xor_b32 v37, 3, v53\n"
    "v_lshl_add_u32 v7, v37, 5, v36\n"
    "v_mul_u32_u24 v36, 0xe0, v38\n"
    "v_lshl_add_u32 v8, v52, 3, v36\n"
    "v_add_u32 v9, 0xe000, v8\n"
    "s_mul_i32 s29, s68, 0xe0\n"
    "s_lshl_b32 s28, s8, 7\n"
    "v_add_u32 v36, s28, v48\n"
    "v_mul_u32_u24 v37, 0x124a, v36\n"
    "v_lshrrev_b32 v37, 16, v37\n"
    "v_mul_u32_u24 v37, 0xd20, v37\n"
    "v_lshl_add_u32 v37, v36, 4, v37\n"
    "v_add_u32 v10, s29, v37\n"
    "v_add_u32 v36, s28, v48\n"
    "v_add_u32 v36, 64, v36\n"
    "v_mul_u32_u24 v37, 0x124a, v36\n"
    "v_lshrrev_b32 v37, 16, v37\n"
    "v_mul_u32_u24 v37, 0xd20, v37\n"
    "v_lshl_add_u32 v37, v36, 4, v37\n"
    "v_add_u32 v11, s29, v37\n"
    "v_add_u32 v36, s28, v48\n"
    "v_add_u32 v36, 0x80, v36\n"
    "v_mul_u32_u24 v37, 0x124a, v36\n"
    "v_lshrrev_b32 v37, 16, v37\n"
    "v_mul_u32_u24 v37, 0xd20, v37\n"
    "v_lshl_add_u32 v37, v36, 4, v37\n"
    "v_add_u32 v12, s29, v37\n"
    "v_add_u32 v36, s28, v48\n"
    "v_add_u32 v36, 0xc0, v36\n"
    "v_mul_u32_u24 v37, 0x124a, v36\n"
    "v_lshrrev_b32 v37, 16, v37\n"
    "v_mul_u32_u24 v37, 0xd20, v37\n"
    "v_lshl_add_u32 v37, v36, 4, v37\n"
    "v_add_u32 v13, s29, v37\n"
    "v_add_u32 v36, s28, v48\n"
    "v_add_u32 v36, 0x100, v36\n"
    "v_mul_u32_u24 v37, 0x124a, v36\n"
    "v_lshrrev_b32 v37, 16, v37\n"
    "v_mul_u32_u24 v37, 0xd20, v37\n"
    "v_lshl_add_u32 v37, v36, 4, v37\n"
    "v_add_u32 v14, s29, v37\n"
    "v_add_u32 v36, s28, v48\n"
    "v_add_u32 v36, 0x140, v36\n"
    "v_mul_u32_u24 v37, 0x124a, v36\n"
    "v_lshrrev_b32 v37, 16, v37\n"
    "v_mul_u32_u24 v37, 0xd20, v37\n"
    "v_lshl_add_u32 v37, v36, 4, v37\n"
    "v_add_u32 v15, s29, v37\n"
    "v_add_u32 v36, s28, v48\n"
    "v_add_u32 v36, 0x180, v36\n"
    "v_mul_u32_u24 v37, 0x124a, v36\n"
    "v_lshrrev_b32 v37, 16, v37\n"
    "v_mul_u32_u24 v37, 0xd20, v37\n"
    "v_lshl_add_u32 v37, v36, 4, v37\n"
    "v_add_u32 v16, s29, v37\n"
    "s_lshr_b32 s88, s73, 9\n"
    "s_add_u32 m0, s9, 0x9000\n"
    "s_add_u32 s31, s73, 0\n"
    "buffer_load_dwordx4 v120, s[32:35], s88 offen lds\n"
    "s_add_u32 m0, s9, 0x8000\n"
    "s_nop 0\n"
    "buffer_load_dwordx4 v121, s[36:39], s31 offen lds\n"
    "v_mov_b32 v38, 0x80000000\n"
    "v_lshlrev_b32 v36, 3, v48\n"
    "v_cmp_gt_u32 vcc, 8, v48\n"
    "s_nop 1\n"
    "v_cndmask_b32 v17, v38, v36, vcc\n"
    "v_lshlrev_b32 v18, 7, v49\n"
    "v_and_b32 v36, 7, v48\n"
    "v_lshlrev_b32 v19, 7, v36\n"
    "v_cmp_gt_u32 vcc, 16, v48\n"
    "s_nop 1\n"
    "v_cndmask_b32 v18, v38, v18, vcc\n"
    "v_cmp_gt_u32 vcc, 8, v48\n"
    "s_nop 1\n"
    "v_cndmask_b32 v19, v38, v19, vcc\n"
    "v_lshlrev_b32 v36, 3, v49\n"
    "v_cmp_eq_u32 vcc, v50, v51\n"
    "s_nop 1\n"
    "v_cndmask_b32 v20, v38, v36, vcc\n"
    "v_lshl_add_u32 v21, v50, 8, v36\n"
    "v_lshlrev_b32 v37, 4, v50\n"
    "v_add_u32 v23, 0x27800, v37\n"
    "v_mov_b32 v28, 0x3f803f80\n"
    "v_mov_b32 v29, 0x3f803f80\n"
    "v_mov_b32 v30, 0x3f803f80\n"
    "v_mov_b32 v31, 0x3f803f80\n"
    "s_mov_b32 s62, 0xaaaaaaaa\n"
    "s_mov_b32 s64, 0xcccccccc\n"
    "s_mov_b32 s63, 0xaaaaaaaa\n"
    "s_mov_b32 s65, 0xcccccccc\n"
    "v_bfe_i32 v37, v48, 3, 1\n"
    "v_xor_b32 v37, v37, v50\n"
    "v_and_b32 v37, 3, v37\n"
    "v_lshl_add_u32 v37, v49, 2, v37\n"
    "v_lshlrev_b32 v37, 4, v37\n"
    "s_add_u32 s28, s9, 0x1000\n"
    "v_add_u32 v112, s28, v37\n"
    "v_xor_b32 v114, 0x400, v112\n"
    "v_lshl_add_u32 v37, v50, 3, v51\n"
    "v_lshlrev_b32 v37, 7, v37\n"
    "v_lshl_add_u32 v37, v52, 3, v37\n"
    "s_lshl1_add_u32 s28, s70, s69\n"
    "v_xor_b32 v39, s28, v53\n"
    "v_lshl_add_u32 v116, v39, 5, v37\n"
    "v_xor_b32 v118, 32, v116\n"
    "v_add_u32 v113, 0x10000, v112\n"
    "v_add_u32 v115, 0x10000, v114\n"
    "v_add_u32 v117, 0x10000, v116\n"
    "v_add_u32 v119, 0x10000, v118\n"
    "s_lshl_b32 s29, s8, 7\n"
    "s_add_u32 s29, s29, 0x27000\n"
    "v_add_u32 v124, s29, v36\n"
    "v_xor_b32 v125, 0x80, v124\n"
    "v_lshlrev_b32 v37, 3, v48\n"
    "v_add_u32 v37, 0x27400, v37\n"
    "v_cndmask_b32 v122, v37, v124, vcc\n"
    "s_lshl_b32 s29, s28, 6\n"
    "s_add_u32 s29, s29, 0x27200\n"
    "v_lshlrev_b32 v37, 4, v50\n"
    "v_add_u32 v123, s29, v37\n"
    "v_xor_b32 v126, 64, v123\n"
    "s_add_u32 m0, s9, 0xafc0\n"
    "s_add_u32 s31, s73, 0x8000\n"
    "buffer_load_dwordx4 v120, s[32:35], s88 offen offset:64 lds\n"
    "s_add_u32 m0, s9, 0xa000\n"
    "s_nop 0\n"
    "buffer_load_dwordx4 v121, s[36:39], s31 offen lds\n"
    "s_lshl_b32 s28, s69, 11\n"
    "s_xor_b32 s29, s28, 0x800\n"
    "v_add_u32 v136, s28, v4\n"
    "v_add_u32 v140, s29, v4\n"
    "v_add_u32 v128, 0x20000, v136\n"
    "v_add_u32 v132, 0x20000, v140\n"
    "v_add_u32 v4, s10, v4\n"
    "v_add_u32 v137, s28, v5\n"
    "v_add_u32 v141, s29, v5\n"
    "v_add_u32 v129, 0x20000, v137\n"
    "v_add_u32 v133, 0x20000, v141\n"
    "v_add_u32 v5, s10, v5\n"
    "v_add_u32 v138, s28, v6\n"
    "v_add_u32 v142, s29, v6\n"
    "v_add_u32 v130, 0x20000, v138\n"
    "v_add_u32 v134, 0x20000, v142\n"
    "v_add_u32 v6, s10, v6\n"
    "v_add_u32 v139, s28, v7\n"
    "v_add_u32 v143, s29, v7\n"
    "v_add_u32 v131, 0x20000, v139\n"
    "v_add_u32 v135, 0x20000, v143\n"
    "v_add_u32 v7, s10, v7\n"
    "s_xor_b32 s28, s8, 0\n"
    "s_lshr_b32 s29, s28, 1\n"
    "s_mul_i32 s29, s29, 0x1c00\n"
    "s_and_b32 s28, s28, 1\n"
    "s_lshl_b32 s28, s28, 10\n"
    "s_add_u32 s28, s28, s29\n"
    "v_add_u32 v32, s28, v1\n"
    "s_xor_b32 s28, s8, 1\n"
    "s_lshr_b32 s29, s28, 1\n"
    "s_mul_i32 s29, s29, 0x1c00\n"
    "s_and_b32 s28, s28, 1\n"
    "s_lshl_b32 s28, s28, 10\n"
    "s_add_u32 s28, s28, s29\n"
    "v_add_u32 v33, s28, v1\n"
    "s_xor_b32 s28, s8, 2\n"
    "s_lshr_b32 s29, s28, 1\n"
    "s_mul_i32 s29, s29, 0x1c00\n"
    "s_and_b32 s28, s28, 1\n"
    "s_lshl_b32 s28, s28, 10\n"
    "s_add_u32 s28, s28, s29\n"
    "v_add_u32 v34, s28, v1\n"
    "s_xor_b32 s28, s8, 3\n"
    "s_lshr_b32 s29, s28, 1\n"
    "s_mul_i32 s29, s29, 0x1c00\n"
    "s_and_b32 s28, s28, 1\n"
    "s_lshl_b32 s28, s28, 10\n"
    "s_add_u32 s28, s28, s29\n"
    "v_add_u32 v35, s28, v1\n"
    "v_lshrrev_b32 v36, 3, v48\n"
    "v_lshlrev_b32 v36, 10, v36\n"
    "s_sub_u32 s28, s25, 0x800\n"
    "v_add_u32 v46, s28, v36\n"
    "v_mov_b32 v36, s15\n"
    "v_mov_b32 v37, s20\n"
    "v_cmp_gt_u32 vcc, 16, v48\n"
    "s_nop 1\n"
    "v_cndmask_b32 v46, v46, v37, vcc\n"
    "v_cmp_gt_u32 vcc, 8, v48\n"
    "s_nop 1\n"
    "v_cndmask_b32 v46, v46, v36, vcc\n"
    "v_and_b32 v36, 0x70, v1\n"
    "v_add_u32 v46, v46, v36\n"
    "v_mov_b32 v38, 0x80000000\n"
    "v_cmp_gt_u32 vcc, 40, v48\n"
    "s_nop 1\n"
    "v_cndmask_b32 v46, v38, v46, vcc\n"
    "s_waitcnt vmcnt(9)\n"
    "s_barrier\n"
    "ds_read_b128 v[48:51], v112\n"
    "ds_read_b64_tr_b16 v[64:65], v116\n"
    "ds_read_b64_tr_b16 v[66:67], v116 offset:512\n"
    "ds_read_b128 v[52:55], v114\n"
    "ds_read_b64_tr_b16 v[68:69], v118\n"
    "ds_read_b64_tr_b16 v[70:71], v118 offset:512\n"
    "ds_read_b128 v[56:59], v112 offset:8192\n"
    "ds_read_b64_tr_b16 v[72:73], v116 offset:8192\n"
    "ds_read_b64_tr_b16 v[74:75], v116 offset:8704\n"
    "ds_read_b128 v[60:63], v114 offset:8192\n"
    "ds_read_b64_tr_b16 v[76:77], v118 offset:8192\n"
    "ds_read_b64_tr_b16 v[78:79], v118 offset:8704\n"
    "s_waitcnt vmcnt(5)\n"
    "s_barrier\n"
    "s_waitcnt lgkmcnt(6)\n"
    "v_mfma_f32_16x16x32_bf16 v[100:103], v[48:51], v[48:51], 0\n"
    "s_add_u32 m0, s9, 0xcf80\n"
    "s_add_u32 s31, s73, 0x10000\n"
    "buffer_load_dwordx4 v120, s[32:35], s88 offen offset:128 lds\n"
    "s_add_u32 m0, s9, 0xc000\n"
    "s_nop 0\n"
    "buffer_load_dwordx4 v121, s[36:39], s31 offen lds\n"
    "v_mfma_f32_16x16x32_bf16 v[96:99], v[28:31], v[48:51], 0\n"
    "ds_read_b128 v[144:147], v112 offset:16384\n"
    "ds_read_b64_tr_b16 v[160:161], v116 offset:16384\n"
    "v_mfma_f32_16x16x32_bf16 v[104:107], v[64:67], v[28:31], 0\n"
    "ds_read_b64_tr_b16 v[162:163], v116 offset:16896\n"
    "ds_read_b128 v[148:151], v114 offset:16384\n"
    "v_mfma_f32_16x16x32_bf16 v[80:83], v[64:67], v[48:51], 0\n"
    "s_add_u32 m0, s9, 0xef40\n"
    "s_add_u32 s31, s73, 0x18000\n"
    "buffer_load_dwordx4 v120, s[32:35], s88 offen offset:192 lds\n"
    "s_add_u32 m0, s9, 0xe000\n"
    "s_nop 0\n"
    "buffer_load_dwordx4 v121, s[36:39], s31 offen lds\n"
    "v_mfma_f32_16x16x32_bf16 v[88:91], v[64:67], v[52:55], 0\n"
    "ds_read_b64_tr_b16 v[164:165], v118 offset:16384\n"
    "ds_read_b64_tr_b16 v[166:167], v118 offset:16896\n"
    "v_mfma_f32_16x16x32_bf16 v[84:87], v[68:71], v[48:51], 0\n"
    "ds_read_b128 v[152:155], v112 offset:24576\n"
    "ds_read_b64_tr_b16 v[168:169], v116 offset:24576\n"
    "v_mfma_f32_16x16x32_bf16 v[92:95], v[68:71], v[52:55], 0\n"
    "ds_read_b64_tr_b16 v[170:171], v116 offset:25088\n"
    "ds_read_b128 v[156:159], v114 offset:24576\n"
    "s_waitcnt lgkmcnt(10)\n"
    "v_mfma_f32_16x16x32_bf16 v[100:103], v[56:59], v[56:59], v[100:103]\n"
    "ds_read_b64_tr_b16 v[172:173], v118 offset:24576\n"
    "ds_read_b64_tr_b16 v[174:175], v118 offset:25088\n"
    "v_mfma_f32_16x16x32_bf16 v[96:99], v[28:31], v[56:59], v[96:99]\n"
    "v_mfma_f32_16x16x32_bf16 v[104:107], v[72:75], v[28:31], v[104:107]\n"
    "v_mfma_f32_16x16x32_bf16 v[80:83], v[72:75], v[56:59], v[80:83]\n"
    "v_mfma_f32_16x16x32_bf16 v[88:91], v[72:75], v[60:63], v[88:91]\n"
    "v_mfma_f32_16x16x32_bf16 v[84:87], v[76:79], v[56:59], v[84:87]\n"
    "v_mfma_f32_16x16x32_bf16 v[92:95], v[76:79], v[60:63], v[92:95]\n"
    "s_waitcnt vmcnt(4)\n"
    "s_barrier\n"
    "s_waitcnt lgkmcnt(6)\n"
    "v_mfma_f32_16x16x32_bf16 v[100:103], v[144:147], v[144:147], v[100:103]\n"
    "s_lshr_b32 s88, s74, 9\n"
    "s_add_u32 m0, s9, 0x11000\n"
    "s_add_u32 s31, s74, 0\n"
    "buffer_load_dwordx4 v120, s[32:35], s88 offen lds\n"
    "s_add_u32 m0, s9, 0x10000\n"
    "s_nop 0\n"
    "buffer_load_dwordx4 v121, s[36:39], s31 offen lds\n"
    "v_mfma_f32_16x16x32_bf16 v[96:99], v[28:31], v[144:147], v[96:99]\n"
    "ds_read_b128 v[48:51], v112 offset:32768\n"
    "ds_read_b64_tr_b16 v[64:65], v116 offset:32768\n"
    "v_mfma_f32_16x16x32_bf16 v[104:107], v[160:163], v[28:31], v[104:107]\n"
    "ds_read_b64_tr_b16 v[66:67], v116 offset:33280\n"
    "ds_read_b128 v[52:55], v114 offset:32768\n"
    "v_mfma_f32_16x16x32_bf16 v[80:83], v[160:163], v[144:147], v[80:83]\n"
    "s_add_u32 m0, s9, 0x12fc0\n"
    "s_add_u32 s31, s74, 0x8000\n"
    "buffer_load_dwordx4 v120, s[32:35], s88 offen offset:64 lds\n"
    "s_add_u32 m0, s9, 0x12000\n"
    "s_nop 0\n"
    "buffer_load_dwordx4 v121, s[36:39], s31 offen lds\n"
    "v_mfma_f32_16x16x32_bf16 v[88:91], v[160:163], v[148:151], v[88:91]\n"
    "ds_read_b64_tr_b16 v[68:69], v118 offset:32768\n"
    "ds_read_b64_tr_b16 v[70:71], v118 offset:33280\n"
    "v_mfma_f32_16x16x32_bf16 v[84:87], v[164:167], v[144:147], v[84:87]\n"
    "ds_read_b128 v[56:59], v112 offset:40960\n"
    "ds_read_b64_tr_b16 v[72:73], v116 offset:40960\n"
    "v_mfma_f32_16x16x32_bf16 v[92:95], v[164:167], v[148:151], v[92:95]\n"
    "ds_read_b64_tr_b16 v[74:75], v116 offset:41472\n"
    "ds_read_b128 v[60:63], v114 offset:40960\n"
    "s_waitcnt lgkmcnt(10)\n"
    "v_mfma_f32_16x16x32_bf16 v[100:103], v[152:155], v[152:155], v[100:103]\n"
    "ds_read_b64_tr_b16 v[76:77], v118 offset:40960\n"
    "ds_read_b64_tr_b16 v[78:79], v118 offset:41472\n"
    "v_mfma_f32_16x16x32_bf16 v[96:99], v[28:31], v[152:155], v[96:99]\n"
    "v_mfma_f32_16x16x32_bf16 v[104:107], v[168:171], v[28:31], v[104:107]\n"
    "v_mfma_f32_16x16x32_bf16 v[80:83], v[168:171], v[152:155], v[80:83]\n"
    "v_mfma_f32_16x16x32_bf16 v[88:91], v[168:171], v[156:159], v[88:91]\n"
    "v_mfma_f32_16x16x32_bf16 v[84:87], v[172:175], v[152:155], v[84:87]\n"
    "v_mfma_f32_16x16x32_bf16 v[92:95], v[172:175], v[156:159], v[92:95]\n"
    "s_waitcnt vmcnt(4)\n"
    "s_barrier\n"
    "s_waitcnt lgkmcnt(6)\n"
    "v_mfma_f32_16x16x32_bf16 v[100:103], v[48:51], v[48:51], v[100:103]\n"
    "s_add_u32 m0, s9, 0x14f80\n"
    "s_add_u32 s31, s74, 0x10000\n"
    "buffer_load_dwordx4 v120, s[32:35], s88 offen offset:128 lds\n"
    "s_add_u32 m0, s9, 0x14000\n"
    "s_nop 0\n"
    "buffer_load_dwordx4 v121, s[36:39], s31 offen lds\n"
    "v_mfma_f32_16x16x32_bf16 v[96:99], v[28:31], v[48:51], v[96:99]\n"
    "ds_read_b128 v[144:147], v112 offset:49152\n"
    "ds_read_b64_tr_b16 v[160:161], v116 offset:49152\n"
    "v_mfma_f32_16x16x32_bf16 v[104:107], v[64:67], v[28:31], v[104:107]\n"
    "ds_read_b64_tr_b16 v[162:163], v116 offset:49664\n"
    "ds_read_b128 v[148:151], v114 offset:49152\n"
    "v_mfma_f32_16x16x32_bf16 v[80:83], v[64:67], v[48:51], v[80:83]\n"
    "s_add_u32 m0, s9, 0x16f40\n"
    "s_add_u32 s31, s74, 0x18000\n"
    "buffer_load_dwordx4 v120, s[32:35], s88 offen offset:192 lds\n"
    "s_add_u32 m0, s9, 0x16000\n"
    "s_nop 0\n"
    "buffer_load_dwordx4 v121, s[36:39], s31 offen lds\n"
    "v_mfma_f32_16x16x32_bf16 v[88:91], v[64:67], v[52:55], v[88:91]\n"
    "ds_read_b64_tr_b16 v[164:165], v118 offset:49152\n"
    "ds_read_b64_tr_b16 v[166:167], v118 offset:49664\n"
    "v_mfma_f32_16x16x32_bf16 v[84:87], v[68:71], v[48:51], v[84:87]\n"
    "ds_read_b128 v[152:155], v112 offset:57344\n"
    "ds_read_b64_tr_b16 v[168:169], v116 offset:57344\n"
    "v_mfma_f32_16x16x32_bf16 v[92:95], v[68:71], v[52:55], v[92:95]\n"
    "ds_read_b64_tr_b16 v[170:171], v116 offset:57856\n"
    "ds_read_b128 v[156:159], v114 offset:57344\n"
    "s_waitcnt lgkmcnt(10)\n"
    "v_mfma_f32_16x16x32_bf16 v[100:103], v[56:59], v[56:59], v[100:103]\n"
    "ds_read_b64_tr_b16 v[172:173], v118 offset:57344\n"
    "ds_read_b64_tr_b16 v[174:175], v118 offset:57856\n"
    "v_mfma_f32_16x16x32_bf16 v[96:99], v[28:31], v[56:59], v[96:99]\n"
    "v_mfma_f32_16x16x32_bf16 v[104:107], v[72:75], v[28:31], v[104:107]\n"
    "v_mfma_f32_16x16x32_bf16 v[80:83], v[72:75], v[56:59], v[80:83]\n"
    "v_mfma_f32_16x16x32_bf16 v[88:91], v[72:75], v[60:63], v[88:91]\n"
    "v_mfma_f32_16x16x32_bf16 v[84:87], v[76:79], v[56:59], v[84:87]\n"
    "v_mfma_f32_16x16x32_bf16 v[92:95], v[76:79], v[60:63], v[92:95]\n"
    "s_waitcnt vmcnt(4)\n"
    "s_barrier\n"
    "s_waitcnt lgkmcnt(6)\n"
    "v_mfma_f32_16x16x32_bf16 v[100:103], v[144:147], v[144:147], v[100:103]\n"
    "s_lshr_b32 s88, s75, 9\n"
    "s_add_u32 m0, s9, 0x19000\n"
    "s_add_u32 s31, s75, 0\n"
    "buffer_load_dwordx4 v120, s[32:35], s88 offen lds\n"
    "s_add_u32 m0, s9, 0x18000\n"
    "s_nop 0\n"
    "buffer_load_dwordx4 v121, s[36:39], s31 offen lds\n"
    "v_mfma_f32_16x16x32_bf16 v[96:99], v[28:31], v[144:147], v[96:99]\n"
    "ds_read_b128 v[48:51], v113\n"
    "ds_read_b64_tr_b16 v[64:65], v117\n"
    "v_mfma_f32_16x16x32_bf16 v[104:107], v[160:163], v[28:31], v[104:107]\n"
    "ds_read_b64_tr_b16 v[66:67], v117 offset:512\n"
    "ds_read_b128 v[52:55], v115\n"
    "v_mfma_f32_16x16x32_bf16 v[80:83], v[160:163], v[144:147], v[80:83]\n"
    "s_add_u32 m0, s9, 0x1afc0\n"
    "s_add_u32 s31, s75, 0x8000\n"
    "buffer_load_dwordx4 v120, s[32:35], s88 offen offset:64 lds\n"
    "s_add_u32 m0, s9, 0x1a000\n"
    "s_nop 0\n"
    "buffer_load_dwordx4 v121, s[36:39], s31 offen lds\n"
    "v_mfma_f32_16x16x32_bf16 v[88:91], v[160:163], v[148:151], v[88:91]\n"
    "ds_read_b64_tr_b16 v[68:69], v119\n"
    "ds_read_b64_tr_b16 v[70:71], v119 offset:512\n"
    "v_mfma_f32_16x16x32_bf16 v[84:87], v[164:167], v[144:147], v[84:87]\n"
    "ds_read_b128 v[56:59], v113 offset:8192\n"
    "ds_read_b64_tr_b16 v[72:73], v117 offset:8192\n"
    "v_mfma_f32_16x16x32_bf16 v[92:95], v[164:167], v[148:151], v[92:95]\n"
    "ds_read_b64_tr_b16 v[74:75], v117 offset:8704\n"
    "ds_read_b128 v[60:63], v115 offset:8192\n"
    "s_waitcnt lgkmcnt(10)\n"
    "v_mfma_f32_16x16x32_bf16 v[100:103], v[152:155], v[152:155], v[100:103]\n"
    "ds_read_b64_tr_b16 v[76:77], v119 offset:8192\n"
    "ds_read_b64_tr_b16 v[78:79], v119 offset:8704\n"
    "v_mfma_f32_16x16x32_bf16 v[96:99], v[28:31], v[152:155], v[96:99]\n"
    "v_mfma_f32_16x16x32_bf16 v[104:107], v[168:171], v[28:31], v[104:107]\n"
    "v_mfma_f32_16x16x32_bf16 v[80:83], v[168:171], v[152:155], v[80:83]\n"
    "v_mfma_f32_16x16x32_bf16 v[88:91], v[168:171], v[156:159], v[88:91]\n"
    "v_mfma_f32_16x16x32_bf16 v[84:87], v[172:175], v[152:155], v[84:87]\n"
    "v_mfma_f32_16x16x32_bf16 v[92:95], v[172:175], v[156:159], v[92:95]\n"
    "s_waitcnt vmcnt(4)\n"
    "s_barrier\n"
    "s_waitcnt lgkmcnt(6)\n"
    "v_mfma_f32_16x16x32_bf16 v[100:103], v[48:51], v[48:51], v[100:103]\n"
    "s_add_u32 m0, s9, 0x1cf80\n"
    "s_add_u32 s31, s75, 0x10000\n"
    "buffer_load_dwordx4 v120, s[32:35], s88 offen offset:128 lds\n"
    "s_add_u32 m0, s9, 0x1c000\n"
    "s_nop 0\n"
    "buffer_load_dwordx4 v121, s[36:39], s31 offen lds\n"
    "v_mfma_f32_16x16x32_bf16 v[96:99], v[28:31], v[48:51], v[96:99]\n"
    "ds_read_b128 v[144:147], v113 offset:16384\n"
    "ds_read_b64_tr_b16 v[160:161], v117 offset:16384\n"
    "v_mfma_f32_16x16x32_bf16 v[104:107], v[64:67], v[28:31], v[104:107]\n"
    "ds_read_b64_tr_b16 v[162:163], v117 offset:16896\n"
    "ds_read_b128 v[148:151], v115 offset:16384\n"
    "v_mfma_f32_16x16x32_bf16 v[80:83], v[64:67], v[48:51], v[80:83]\n"
    "s_add_u32 m0, s9, 0x1ef40\n"
    "s_add_u32 s31, s75, 0x18000\n"
    "buffer_load_dwordx4 v120, s[32:35], s88 offen offset:192 lds\n"
    "s_add_u32 m0, s9, 0x1e000\n"
    "s_nop 0\n"
    "buffer_load_dwordx4 v121, s[36:39], s31 offen lds\n"
    "v_mfma_f32_16x16x32_bf16 v[88:91], v[64:67], v[52:55], v[88:91]\n"
    "ds_read_b64_tr_b16 v[164:165], v119 offset:16384\n"
    "ds_read_b64_tr_b16 v[166:167], v119 offset:16896\n"
    "v_mfma_f32_16x16x32_bf16 v[84:87], v[68:71], v[48:51], v[84:87]\n"
    "ds_read_b128 v[152:155], v113 offset:24576\n"
    "ds_read_b64_tr_b16 v[168:169], v117 offset:24576\n"
    "v_mfma_f32_16x16x32_bf16 v[92:95], v[68:71], v[52:55], v[92:95]\n"
    "s_add_u32 m0, s9, 0x20000\n"
    "s_add_u32 s31, s72, 0\n"
    "buffer_load_dwordx4 v127, s[40:43], s31 offen lds\n"
    "s_waitcnt lgkmcnt(8)\n"
    "v_mfma_f32_16x16x32_bf16 v[100:103], v[56:59], v[56:59], v[100:103]\n"
    "ds_read_b64_tr_b16 v[170:171], v117 offset:25088\n"
    "ds_read_b128 v[156:159], v115 offset:24576\n"
    "v_mfma_f32_16x16x32_bf16 v[96:99], v[28:31], v[56:59], v[96:99]\n"
    "ds_read_b64_tr_b16 v[172:173], v119 offset:24576\n"
    "ds_read_b64_tr_b16 v[174:175], v119 offset:25088\n"
    "v_mfma_f32_16x16x32_bf16 v[104:107], v[72:75], v[28:31], v[104:107]\n"
    "s_add_u32 m0, s9, 0x21000\n"
    "s_add_u32 s31, s72, 0x8000\n"
    "buffer_load_dwordx4 v127, s[40:43], s31 offen lds\n"
    "v_mfma_f32_16x16x32_bf16 v[80:83], v[72:75], v[56:59], v[80:83]\n"
    "v_mfma_f32_16x16x32_bf16 v[88:91], v[72:75], v[60:63], v[88:91]\n"
    "v_mfma_f32_16x16x32_bf16 v[84:87], v[76:79], v[56:59], v[84:87]\n"
    "v_mov_b32 v36, 0x9e3779b1\n"
    "v_mul_lo_u32 v24, v26, v36\n"
    "v_mul_lo_u32 v25, v27, v36\n"
    "v_add_u32 v24, 0x7f4a7c15, v24\n"
    "v_add_u32 v25, 0x2545f491, v25\n"
    "v_xor_b32 v40, 1, v24\n"
    "buffer_store_dwordx4 v[40:43], v46, s[56:59], 0 offen sc1\n"
    "v_mfma_f32_16x16x32_bf16 v[92:95], v[76:79], v[60:63], v[92:95]\n"
    "s_waitcnt vmcnt(7)\n"
    "s_barrier\n"
    "s_waitcnt lgkmcnt(6)\n"
    "v_mfma_f32_16x16x32_bf16 v[100:103], v[144:147], v[144:147], v[100:103]\n"
    "s_add_u32 m0, s9, 0x22000\n"
    "s_add_u32 s31, s72, 0x10000\n"
    "buffer_load_dwordx4 v127, s[40:43], s31 offen lds\n"
    "v_mfma_f32_16x16x32_bf16 v[96:99], v[28:31], v[144:147], v[96:99]\n"
    "ds_read_b128 v[48:51], v113 offset:32768\n"
    "ds_read_b64_tr_b16 v[64:65], v117 offset:32768\n"
    "v_mfma_f32_16x16x32_bf16 v[104:107], v[160:163], v[28:31], v[104:107]\n"
    "ds_read_b64_tr_b16 v[66:67], v117 offset:33280\n"
    "ds_read_b128 v[52:55], v115 offset:32768\n"
    "v_mfma_f32_16x16x32_bf16 v[80:83], v[160:163], v[144:147], v[80:83]\n"
    "s_add_u32 m0, s9, 0x23000\n"
    "s_add_u32 s31, s72, 0x18000\n"
    "buffer_load_dwordx4 v127, s[40:43], s31 offen lds\n"
    "v_mfma_f32_16x16x32_bf16 v[88:91], v[160:163], v[148:151], v[88:91]\n"
    "ds_read_b64_tr_b16 v[68:69], v119 offset:32768\n"
    "ds_read_b64_tr_b16 v[70:71], v119 offset:33280\n"
    "v_mfma_f32_16x16x32_bf16 v[84:87], v[164:167], v[144:147], v[84:87]\n"
    "ds_read_b128 v[56:59], v113 offset:40960\n"
    "ds_read_b64_tr_b16 v[72:73], v117 offset:40960\n"
    "v_mfma_f32_16x16x32_bf16 v[92:95], v[164:167], v[148:151], v[92:95]\n"
    "s_add_u32 m0, s9, 0x24000\n"
    "s_add_u32 s31, s73, 0\n"
    "buffer_load_dwordx4 v127, s[40:43], s31 offen lds\n"
    "s_waitcnt lgkmcnt(8)\n"
    "v_mfma_f32_16x16x32_bf16 v[100:103], v[152:155], v[152:155], v[100:103]\n"
    "ds_read_b64_tr_b16 v[74:75], v117 offset:41472\n"
    "ds_read_b128 v[60:63], v115 offset:40960\n"
    "v_mfma_f32_16x16x32_bf16 v[96:99], v[28:31], v[152:155], v[96:99]\n"
    "ds_read_b64_tr_b16 v[76:77], v119 offset:40960\n"
    "ds_read_b64_tr_b16 v[78:79], v119 offset:41472\n"
    "v_mfma_f32_16x16x32_bf16 v[104:107], v[168:171], v[28:31], v[104:107]\n"
    "s_add_u32 m0, s9, 0x25000\n"
    "s_add_u32 s31, s73, 0x8000\n"
    "buffer_load_dwordx4 v127, s[40:43], s31 offen lds\n"
    "v_mfma_f32_16x16x32_bf16 v[80:83], v[168:171], v[152:155], v[80:83]\n"
    "v_mfma_f32_16x16x32_bf16 v[88:91], v[168:171], v[156:159], v[88:91]\n"
    "v_mfma_f32_16x16x32_bf16 v[84:87], v[172:175], v[152:155], v[84:87]\n"
    "s_add_u32 m0, s9, 0x26000\n"
    "s_add_u32 s31, s73, 0x10000\n"
    "buffer_load_dwordx4 v127, s[40:43], s31 offen lds\n"
    "v_mfma_f32_16x16x32_bf16 v[92:95], v[172:175], v[156:159], v[92:95]\n"
    "s_add_u32 m0, s9, 0\n"
    "s_add_u32 s31, s73, 0x18000\n"
    "buffer_load_dwordx4 v127, s[40:43], s31 offen lds\n"
    "s_waitcnt vmcnt(9)\n"
    "s_barrier\n"
    "s_waitcnt lgkmcnt(6)\n"
    "v_mfma_f32_16x16x32_bf16 v[100:103], v[48:51], v[48:51], v[100:103]\n"
    "s_add_u32 m0, s9, 0x1000\n"
    "s_add_u32 s31, s74, 0\n"
    "buffer_load_dwordx4 v127, s[40:43], s31 offen lds\n"
    "v_mfma_f32_16x16x32_bf16 v[96:99], v[28:31], v[48:51], v[96:99]\n"
    "ds_read_b128 v[144:147], v113 offset:49152\n"
    "ds_read_b64_tr_b16 v[160:161], v117 offset:49152\n"
    "v_mfma_f32_16x16x32_bf16 v[104:107], v[64:67], v[28:31], v[104:107]\n"
    "ds_read_b64_tr_b16 v[162:163], v117 offset:49664\n"
    "ds_read_b128 v[148:151], v115 offset:49152\n"
    "v_mfma_f32_16x16x32_bf16 v[80:83], v[64:67], v[48:51], v[80:83]\n"
    "s_add_u32 m0, s9, 0x2000\n"
    "s_add_u32 s31, s74, 0x8000\n"
    "buffer_load_dwordx4 v127, s[40:43], s31 offen lds\n"
    "v_mfma_f32_16x16x32_bf16 v[88:91], v[64:67], v[52:55], v[88:91]\n"
    "ds_read_b64_tr_b16 v[164:165], v119 offset:49152\n"
    "ds_read_b64_tr_b16 v[166:167], v119 offset:49664\n"
    "v_mfma_f32_16x16x32_bf16 v[84:87], v[68:71], v[48:51], v[84:87]\n"
    "ds_read_b128 v[152:155], v113 offset:57344\n"
    "ds_read_b64_tr_b16 v[168:169], v117 offset:57344\n"
    "v_mfma_f32_16x16x32_bf16 v[92:95], v[68:71], v[52:55], v[92:95]\n"
    "s_add_u32 m0, s9, 0x3000\n"
    "s_add_u32 s31, s74, 0x10000\n"
    "buffer_load_dwordx4 v127, s[40:43], s31 offen lds\n"
    "s_waitcnt lgkmcnt(8)\n"
    "v_mfma_f32_16x16x32_bf16 v[100:103], v[56:59], v[56:59], v[100:103]\n"
    "ds_read_b64_tr_b16 v[170:171], v117 offset:57856\n"
    "ds_read_b128 v[156:159], v115 offset:57344\n"
    "v_mfma_f32_16x16x32_bf16 v[96:99], v[28:31], v[56:59], v[96:99]\n"
    "ds_read_b64_tr_b16 v[172:173], v119 offset:57344\n"
    "ds_read_b64_tr_b16 v[174:175], v119 offset:57856\n"
    "v_mfma_f32_16x16x32_bf16 v[104:107], v[72:75], v[28:31], v[104:107]\n"
    "s_add_u32 m0, s9, 0x4000\n"
    "s_add_u32 s31, s74, 0x18000\n"
    "buffer_load_dwordx4 v127, s[40:43], s31 offen lds\n"
    "v_mfma_f32_16x16x32_bf16 v[80:83], v[72:75], v[56:59], v[80:83]\n"
    "v_mfma_f32_16x16x32_bf16 v[88:91], v[72:75], v[60:63], v[88:91]\n"
    "v_mfma_f32_16x16x32_bf16 v[84:87], v[76:79], v[56:59], v[84:87]\n"
    "s_add_u32 m0, s9, 0x5000\n"
    "s_add_u32 s31, s75, 0\n"
    "buffer_load_dwordx4 v127, s[40:43], s31 offen lds\n"
    "v_mfma_f32_16x16x32_bf16 v[92:95], v[76:79], v[60:63], v[92:95]\n"
    "s_add_u32 m0, s9, 0x6000\n"
    "s_add_u32 s31, s75, 0x8000\n"
    "buffer_load_dwordx4 v127, s[40:43], s31 offen lds\n"
    "s_waitcnt lgkmcnt(6)\n"
    "v_mfma_f32_16x16x32_bf16 v[100:103], v[144:147], v[144:147], v[100:103]\n"
    "s_add_u32 m0, s9, 0x7000\n"
    "s_add_u32 s31, s75, 0x10000\n"
    "buffer_load_dwordx4 v127, s[40:43], s31 offen lds\n"
    "v_mfma_f32_16x16x32_bf16 v[96:99], v[28:31], v[144:147], v[96:99]\n"
    "v_mfma_f32_16x16x32_bf16 v[104:107], v[160:163], v[28:31], v[104:107]\n"
    "v_mfma_f32_16x16x32_bf16 v[80:83], v[160:163], v[144:147], v[80:83]\n"
    "s_add_u32 m0, s9, 0x8000\n"
    "s_add_u32 s31, s75, 0x18000\n"
    "buffer_load_dwordx4 v127, s[40:43], s31 offen lds\n"
    "v_mfma_f32_16x16x32_bf16 v[88:91], v[160:163], v[148:151], v[88:91]\n"
    "v_mfma_f32_16x16x32_bf16 v[84:87], v[164:167], v[144:147], v[84:87]\n"
    "v_mfma_f32_16x16x32_bf16 v[92:95], v[164:167], v[148:151], v[92:95]\n"
    "buffer_load_dwordx4 v[40:43], v22, s[32:35], 0 offen\n"
    "s_waitcnt lgkmcnt(0)\n"
    "v_mfma_f32_16x16x32_bf16 v[100:103], v[152:155], v[152:155], v[100:103]\n"
    "v_mfma_f32_16x16x32_bf16 v[96:99], v[28:31], v[152:155], v[96:99]\n"
    "v_mfma_f32_16x16x32_bf16 v[104:107], v[168:171], v[28:31], v[104:107]\n"
    "buffer_load_dwordx4 v[44:47], v22, s[32:35], 0 offen offset:64\n"
    "v_mfma_f32_16x16x32_bf16 v[80:83], v[168:171], v[152:155], v[80:83]\n"
    "v_mfma_f32_16x16x32_bf16 v[88:91], v[168:171], v[156:159], v[88:91]\n"
    "v_mfma_f32_16x16x32_bf16 v[84:87], v[172:175], v[152:155], v[84:87]\n"
    "v_mfma_f32_16x16x32_bf16 v[92:95], v[172:175], v[156:159], v[92:95]\n"
    "buffer_load_dword v109, v110, s[44:47], 0 offen\n"
    "buffer_load_dword v109, v111, s[48:51], 0 offen\n"
    "v_cndmask_b32_e64 v100, v100, v101, s[62:63]\n"
    "v_cndmask_b32_e64 v102, v102, v103, s[62:63]\n"
    "v_cndmask_b32_e64 v97, v100, v102, s[64:65]\n"
    "ds_write_b64 v122, v[96:97]\n"
    "ds_write_b128 v123, v[104:107]\n"
    "s_waitcnt lgkmcnt(0)\n"
    "s_barrier\n"
    "ds_read_b64 v[144:145], v124\n"
    "ds_read_b64 v[146:147], v125\n"
    "ds_read_b128 v[148:151], v126\n"
    "s_waitcnt lgkmcnt(1)\n"
    "v_mul_f32 v152, 0x3b000000, v144\n"
    "v_mul_f32 v154, 0x3b000000, v146\n"
    "v_mul_f32 v160, 0x3b000000, v145\n"
    "v_mul_f32 v161, 0x3b000000, v147\n"
    "v_fma_f32 v160, -v152, v152, v160\n"
    "v_fma_f32 v161, -v154, v154, v161\n"
    "v_max_f32 v160, 0, v160\n"
    "v_max_f32 v161, 0, v161\n"
    "v_add_f32 v160, 0x3727c5ac, v160\n"
    "v_add_f32 v161, 0x3727c5ac, v161\n"
    "v_rsq_f32 v157, v160\n"
    "v_rsq_f32 v159, v161\n"
    "v_mul_f32 v153, v160, v157\n"
    "v_mul_f32 v155, v161, v159\n"
    "v_mul_f32 v156, 0xc0135761, v157\n"
    "v_mul_f32 v158, 0xc0135761, v159\n"
    "v_mul_f32 v157, v157, v157\n"
    "v_mul_f32 v159, v159, v159\n"
    "v_mul_f32 v157, 0x3d372713, v157\n"
    "v_mul_f32 v159, 0x3d372713, v159\n"
    "v_mul_f32 v157, v157, v156\n"
    "v_mul_f32 v159, v159, v158\n"
    "s_waitcnt lgkmcnt(0)\n"
    "v_pk_fma_f32 v[80:81], v[152:153], v[104:105], v[80:81] op_sel_hi:[0,1,1] neg_lo:[1,0,0] neg_hi:[1,0,0]\n"
    "v_pk_fma_f32 v[82:83], v[152:153], v[106:107], v[82:83] op_sel_hi:[0,1,1] neg_lo:[1,0,0] neg_hi:[1,0,0]\n"
    "v_pk_fma_f32 v[84:85], v[152:153], v[148:149], v[84:85] op_sel_hi:[0,1,1] neg_lo:[1,0,0] neg_hi:[1,0,0]\n"
    "v_pk_fma_f32 v[86:87], v[152:153], v[150:151], v[86:87] op_sel_hi:[0,1,1] neg_lo:[1,0,0] neg_hi:[1,0,0]\n"
    "v_pk_mul_f32 v[160:161], v[80:81], v[80:81]\n"
    "v_pk_mul_f32 v[162:163], v[82:83], v[82:83]\n"
    "v_pk_mul_f32 v[164:165], v[84:85], v[84:85]\n"
    "v_pk_mul_f32 v[166:167], v[86:87], v[86:87]\n"
    "v_pk_fma_f32 v[160:161], v[160:161], v[156:157], v[156:157] op_sel:[0,1,0] op_sel_hi:[1,1,0]\n"
    "v_pk_fma_f32 v[162:163], v[162:163], v[156:157], v[156:157] op_sel:[0,1,0] op_sel_hi:[1,1,0]\n"
    "v_pk_fma_f32 v[164:165], v[164:165], v[156:157], v[156:157] op_sel:[0,1,0] op_sel_hi:[1,1,0]\n"
    "v_pk_fma_f32 v[166:167], v[166:167], v[156:157], v[156:157] op_sel:[0,1,0] op_sel_hi:[1,1,0]\n"
    "v_pk_mul_f32 v[160:161], v[160:161], v[80:81]\n"
    "v_pk_mul_f32 v[162:163], v[162:163], v[82:83]\n"
    "v_pk_mul_f32 v[164:165], v[164:165], v[84:85]\n"
    "v_pk_mul_f32 v[166:167], v[166:167], v[86:87]\n"
    "v_exp_f32 v160, v160\n"
    "v_exp_f32 v161, v161\n"
    "v_exp_f32 v162, v162\n"
    "v_exp_f32 v163, v163\n"
    "v_exp_f32 v164, v164\n"
    "v_exp_f32 v165, v165\n"
    "v_exp_f32 v166, v166\n"
    "v_exp_f32 v167, v167\n"
    "v_pk_fma_f32 v[160:161], v[160:161], v[152:153], v[152:153] op_sel:[0,1,1] op_sel_hi:[1,1,1]\n"
    "v_pk_fma_f32 v[162:163], v[162:163], v[152:153], v[152:153] op_sel:[0,1,1] op_sel_hi:[1,1,1]\n"
    "v_pk_fma_f32 v[164:165], v[164:165], v[152:153], v[152:153] op_sel:[0,1,1] op_sel_hi:[1,1,1]\n"
    "v_pk_fma_f32 v[166:167], v[166:167], v[152:153], v[152:153] op_sel:[0,1,1] op_sel_hi:[1,1,1]\n"
    "v_rcp_f32 v160, v160\n"
    "v_rcp_f32 v161, v161\n"
    "v_rcp_f32 v162, v162\n"
    "v_rcp_f32 v163, v163\n"
    "v_rcp_f32 v164, v164\n"
    "v_rcp_f32 v165, v165\n"
    "v_rcp_f32 v166, v166\n"
    "v_rcp_f32 v167, v167\n"
    "v_pk_mul_f32 v[80:81], v[80:81], v[160:161]\n"
    "v_pk_mul_f32 v[82:83], v[82:83], v[162:163]\n"
    "v_pk_mul_f32 v[84:85], v[84:85], v[164:165]\n"
    "v_pk_mul_f32 v[86:87], v[86:87], v[166:167]\n"
    "v_cvt_pk_bf16_f32 v80, v80, v81\n"
    "v_cvt_pk_bf16_f32 v81, v82, v83\n"
    "v_cvt_pk_bf16_f32 v82, v84, v85\n"
    "v_cvt_pk_bf16_f32 v83, v86, v87\n"
    "buffer_store_dwordx4 v[80:83], v1, s[56:59], s13 offen sc1\n"
    "v_pk_fma_f32 v[88:89], v[154:155], v[104:105], v[88:89] op_sel_hi:[0,1,1] neg_lo:[1,0,0] neg_hi:[1,0,0]\n"
    "v_pk_fma_f32 v[90:91], v[154:155], v[106:107], v[90:91] op_sel_hi:[0,1,1] neg_lo:[1,0,0] neg_hi:[1,0,0]\n"
    "v_pk_fma_f32 v[92:93], v[154:155], v[148:149], v[92:93] op_sel_hi:[0,1,1] neg_lo:[1,0,0] neg_hi:[1,0,0]\n"
    "v_pk_fma_f32 v[94:95], v[154:155], v[150:151], v[94:95] op_sel_hi:[0,1,1] neg_lo:[1,0,0] neg_hi:[1,0,0]\n"
    "v_pk_mul_f32 v[168:169], v[88:89], v[88:89]\n"
    "v_pk_mul_f32 v[170:171], v[90:91], v[90:91]\n"
    "v_pk_mul_f32 v[172:173], v[92:93], v[92:93]\n"
    "v_pk_mul_f32 v[174:175], v[94:95], v[94:95]\n"
    "v_pk_fma_f32 v[168:169], v[168:169], v[158:159], v[158:159] op_sel:[0,1,0] op_sel_hi:[1,1,0]\n"
    "v_pk_fma_f32 v[170:171], v[170:171], v[158:159], v[158:159] op_sel:[0,1,0] op_sel_hi:[1,1,0]\n"
    "v_pk_fma_f32 v[172:173], v[172:173], v[158:159], v[158:159] op_sel:[0,1,0] op_sel_hi:[1,1,0]\n"
    "v_pk_fma_f32 v[174:175], v[174:175], v[158:159], v[158:159] op_sel:[0,1,0] op_sel_hi:[1,1,0]\n"
    "v_pk_mul_f32 v[168:169], v[168:169], v[88:89]\n"
    "v_pk_mul_f32 v[170:171], v[170:171], v[90:91]\n"
    "v_pk_mul_f32 v[172:173], v[172:173], v[92:93]\n"
    "v_pk_mul_f32 v[174:175], v[174:175], v[94:95]\n"
    "v_exp_f32 v168, v168\n"
    "v_exp_f32 v169, v169\n"
    "v_exp_f32 v170, v170\n"
    "v_exp_f32 v171, v171\n"
    "v_exp_f32 v172, v172\n"
    "v_exp_f32 v173, v173\n"
    "v_exp_f32 v174, v174\n"
    "v_exp_f32 v175, v175\n"
    "v_pk_fma_f32 v[168:169], v[168:169], v[154:155], v[154:155] op_sel:[0,1,1] op_sel_hi:[1,1,1]\n"
    "v_pk_fma_f32 v[170:171], v[170:171], v[154:155], v[154:155] op_sel:[0,1,1] op_sel_hi:[1,1,1]\n"
    "v_pk_fma_f32 v[172:173], v[172:173], v[154:155], v[154:155] op_sel:[0,1,1] op_sel_hi:[1,1,1]\n"
    "v_pk_fma_f32 v[174:175], v[174:175], v[154:155], v[154:155] op_sel:[0,1,1] op_sel_hi:[1,1,1]\n"
    "v_rcp_f32 v168, v168\n"
    "v_rcp_f32 v169, v169\n"
    "v_rcp_f32 v170, v170\n"
    "v_rcp_f32 v171, v171\n"
    "v_rcp_f32 v172, v172\n"
    "v_rcp_f32 v173, v173\n"
    "v_rcp_f32 v174, v174\n"
    "v_rcp_f32 v175, v175\n"
    "v_pk_mul_f32 v[88:89], v[88:89], v[168:169]\n"
    "v_pk_mul_f32 v[90:91], v[90:91], v[170:171]\n"
    "v_pk_mul_f32 v[92:93], v[92:93], v[172:173]\n"
    "v_pk_mul_f32 v[94:95], v[94:95], v[174:175]\n"
    "v_cvt_pk_bf16_f32 v88, v88, v89\n"
    "v_cvt_pk_bf16_f32 v89, v90, v91\n"
    "v_cvt_pk_bf16_f32 v90, v92, v93\n"
    "v_cvt_pk_bf16_f32 v91, v94, v95\n"
    "buffer_store_dwordx4 v[88:91], v1, s[56:59], s14 offen sc1\n"
    "s_add_u32 s28, s90, 0\n"
    "s_and_b32 s28, s28, 3\n"
    "s_sub_u32 s28, s28, 0\n"
    "s_mul_i32 s32, s28, 0x70000\n"
    "s_add_u32 s28, s90, 1\n"
    "s_and_b32 s28, s28, 3\n"
    "s_sub_u32 s28, s28, 1\n"
    "s_mul_i32 s34, s28, 0x70000\n"
    "s_add_u32 s28, s90, 2\n"
    "s_and_b32 s28, s28, 3\n"
    "s_sub_u32 s28, s28, 2\n"
    "s_mul_i32 s35, s28, 0x70000\n"
    "s_add_u32 s28, s90, 3\n"
    "s_and_b32 s28, s28, 3\n"
    "s_sub_u32 s28, s28, 3\n"
    "s_mul_i32 s37, s28, 0x70000\n"
    "s_cmp_ge_u32 s8, 2\n"
    "s_cselect_b32 s33, s34, s32\n"
    "s_cselect_b32 s36, s37, s35\n"
    "s_waitcnt vmcnt(6)\n"
    "s_barrier\n"
    "ds_read_b64_tr_b16 a[96:97], v128\n"
    "ds_read_b64_tr_b16 a[98:99], v132\n"
    "ds_read_b64_tr_b16 a[100:101], v129\n"
    "ds_read_b64_tr_b16 a[102:103], v133\n"
    "ds_read_b64_tr_b16 a[104:105], v130\n"
    "ds_read_b64_tr_b16 a[106:107], v134\n"
    "ds_read_b64_tr_b16 a[108:109], v131\n"
    "ds_read_b64_tr_b16 a[110:111], v135\n"
    "ds_read_b64_tr_b16 a[112:113], v128 offset:4096\n"
    "ds_read_b64_tr_b16 a[114:115], v132 offset:4096\n"
    "ds_read_b64_tr_b16 a[116:117], v129 offset:4096\n"
    "ds_read_b64_tr_b16 a[118:119], v133 offset:4096\n"
    "ds_read_b64_tr_b16 a[120:121], v130 offset:4096\n"
    "ds_read_b64_tr_b16 a[122:123], v134 offset:4096\n"
    "ds_read_b64_tr_b16 a[124:125], v131 offset:4096\n"
    "ds_read_b64_tr_b16 a[126:127], v135 offset:4096\n"
    "ds_read_b64_tr_b16 a[128:129], v128 offset:8192\n"
    "ds_read_b64_tr_b16 a[130:131], v132 offset:8192\n"
    "ds_read_b64_tr_b16 a[132:133], v129 offset:8192\n"
    "ds_read_b64_tr_b16 a[134:135], v133 offset:8192\n"
    "ds_read_b64_tr_b16 a[136:137], v130 offset:8192\n"
    "ds_read_b64_tr_b16 a[138:139], v134 offset:8192\n"
    "ds_read_b64_tr_b16 a[140:141], v131 offset:8192\n"
    "ds_read_b64_tr_b16 a[142:143], v135 offset:8192\n"
    "ds_read_b64_tr_b16 a[144:145], v128 offset:12288\n"
    "ds_read_b64_tr_b16 a[146:147], v132 offset:12288\n"
    "ds_read_b64_tr_b16 a[148:149], v129 offset:12288\n"
    "ds_read_b64_tr_b16 a[150:151], v133 offset:12288\n"
    "ds_read_b64_tr_b16 a[152:153], v130 offset:12288\n"
    "ds_read_b64_tr_b16 a[154:155], v134 offset:12288\n"
    "ds_read_b64_tr_b16 a[156:157], v131 offset:12288\n"
    "ds_read_b64_tr_b16 a[158:159], v135 offset:12288\n"
    "ds_read_b64_tr_b16 a[160:161], v128 offset:16384\n"
    "ds_read_b64_tr_b16 a[162:163], v132 offset:16384\n"
    "ds_read_b64_tr_b16 a[164:165], v129 offset:16384\n"
    "ds_read_b64_tr_b16 a[166:167], v133 offset:16384\n"
    "ds_read_b64_tr_b16 a[168:169], v130 offset:16384\n"
    "ds_read_b64_tr_b16 a[170:171], v134 offset:16384\n"
    "ds_read_b64_tr_b16 a[172:173], v131 offset:16384\n"
    "ds_read_b64_tr_b16 a[174:175], v135 offset:16384\n"
    "ds_read_b64_tr_b16 a[176:177], v128 offset:20480\n"
    "ds_read_b64_tr_b16 a[178:179], v132 offset:20480\n"
    "ds_read_b64_tr_b16 a[180:181], v129 offset:20480\n"
    "ds_read_b64_tr_b16 a[182:183], v133 offset:20480\n"
    "ds_read_b64_tr_b16 a[184:185], v130 offset:20480\n"
    "ds_read_b64_tr_b16 a[186:187], v134 offset:20480\n"
    "ds_read_b64_tr_b16 a[188:189], v131 offset:20480\n"
    "ds_read_b64_tr_b16 a[190:191], v135 offset:20480\n"
    "ds_read_b64_tr_b16 a[192:193], v128 offset:24576\n"
    "ds_read_b64_tr_b16 a[194:195], v132 offset:24576\n"
    "ds_read_b64_tr_b16 a[196:197], v129 offset:24576\n"
    "ds_read_b64_tr_b16 a[198:199], v133 offset:24576\n"
    "ds_read_b64_tr_b16 a[200:201], v130 offset:24576\n"
    "ds_read_b64_tr_b16 a[202:203], v134 offset:24576\n"
    "ds_read_b64_tr_b16 a[204:205], v131 offset:24576\n"
    "ds_read_b64_tr_b16 a[206:207], v135 offset:24576\n"
    "ds_read_b64_tr_b16 a[208:209], v136\n"
    "ds_read_b64_tr_b16 a[210:211], v140\n"
    "ds_read_b64_tr_b16 a[212:213], v137\n"
    "ds_read_b64_tr_b16 a[214:215], v141\n"
    "ds_read_b64_tr_b16 a[216:217], v138\n"
    "ds_read_b64_tr_b16 a[218:219], v142\n"
    "ds_read_b64_tr_b16 a[220:221], v139\n"
    "ds_read_b64_tr_b16 a[222:223], v143\n"
    "ds_read_b64_tr_b16 a[224:225], v136 offset:4096\n"
    "ds_read_b64_tr_b16 a[226:227], v140 offset:4096\n"
    "ds_read_b64_tr_b16 a[228:229], v137 offset:4096\n"
    "ds_read_b64_tr_b16 a[230:231], v141 offset:4096\n"
    "ds_read_b64_tr_b16 a[232:233], v138 offset:4096\n"
    "ds_read_b64_tr_b16 a[234:235], v142 offset:4096\n"
    "ds_read_b64_tr_b16 a[236:237], v139 offset:4096\n"
    "ds_read_b64_tr_b16 a[238:239], v143 offset:4096\n"
    "ds_read_b64_tr_b16 a[240:241], v136 offset:8192\n"
    "ds_read_b64_tr_b16 a[242:243], v140 offset:8192\n"
    "ds_read_b64_tr_b16 a[244:245], v137 offset:8192\n"
    "ds_read_b64_tr_b16 a[246:247], v141 offset:8192\n"
    "ds_read_b64_tr_b16 a[248:249], v138 offset:8192\n"
    "ds_read_b64_tr_b16 a[250:251], v142 offset:8192\n"
    "ds_read_b64_tr_b16 a[252:253], v139 offset:8192\n"
    "ds_read_b64_tr_b16 a[254:255], v143 offset:8192\n"
    "s_waitcnt vmcnt(0)\n"
    "buffer_store_dwordx2 v[24:25], v17, s[56:59], s15 offen sc1\n"
    "ds_read_b64_tr_b16 v[160:161], v136 offset:12288\n"
    "ds_read_b64_tr_b16 v[162:163], v140 offset:12288\n"
    "s_add_u32 m0, s10, 0xa000\n"
    "s_add_u32 s31, s34, 0xa8000\n"
    "buffer_load_dwordx4 v15, s[44:47], s31 offen lds\n"
    "ds_read_b64_tr_b16 v[164:165], v137 offset:12288\n"
    "ds_read_b64_tr_b16 v[166:167], v141 offset:12288\n"
    "s_add_u32 m0, s10, 0xa400\n"
    "s_add_u32 s31, s34, 0xa8000\n"
    "buffer_load_dwordx4 v16, s[44:47], s31 offen lds\n"
    "ds_read_b64_tr_b16 v[168:169], v138 offset:12288\n"
    "ds_read_b64_tr_b16 v[170:171], v142 offset:12288\n"
    "s_add_u32 m0, s10, 0xc000\n"
    "s_add_u32 s31, s34, 0xc4000\n"
    "buffer_load_dwordx4 v16, s[44:47], s31 offen lds\n"
    "ds_read_b64_tr_b16 v[172:173], v139 offset:12288\n"
    "ds_read_b64_tr_b16 v[174:175], v143 offset:12288\n"
    "s_add_u32 m0, s10, 0xc400\n"
    "s_add_u32 s31, s34, 0xe0000\n"
    "buffer_load_dwordx4 v10, s[44:47], s31 offen lds\n"
    "ds_read_b64_tr_b16 v[176:177], v136 offset:16384\n"
    "ds_read_b64_tr_b16 v[178:179], v140 offset:16384\n"
    "s_add_u32 m0, s10, 0xe000\n"
    "s_add_u32 s31, s35, 0xfc000\n"
    "buffer_load_dwordx4 v10, s[44:47], s31 offen lds\n"
    "ds_read_b64_tr_b16 v[180:181], v137 offset:16384\n"
    "ds_read_b64_tr_b16 v[182:183], v141 offset:16384\n"
    "s_add_u32 m0, s10, 0xe400\n"
    "s_add_u32 s31, s35, 0xfc000\n"
    "buffer_load_dwordx4 v11, s[44:47], s31 offen lds\n"
    "ds_read_b64_tr_b16 v[184:185], v138 offset:16384\n"
    "ds_read_b64_tr_b16 v[186:187], v142 offset:16384\n"
    "s_add_u32 m0, s10, 0x10000\n"
    "s_add_u32 s31, s35, 0x118000\n"
    "buffer_load_dwordx4 v11, s[44:47], s31 offen lds\n"
    "ds_read_b64_tr_b16 v[188:189], v139 offset:16384\n"
    "ds_read_b64_tr_b16 v[190:191], v143 offset:16384\n"
    "s_add_u32 m0, s10, 0x10400\n"
    "s_add_u32 s31, s35, 0x118000\n"
    "buffer_load_dwordx4 v12, s[44:47], s31 offen lds\n"
    "ds_read_b64_tr_b16 v[192:193], v136 offset:20480\n"
    "ds_read_b64_tr_b16 v[194:195], v140 offset:20480\n"
    "s_add_u32 m0, s10, 0x12000\n"
    "s_add_u32 s31, s35, 0x134000\n"
    "buffer_load_dwordx4 v12, s[44:47], s31 offen lds\n"
    "ds_read_b64_tr_b16 v[196:197], v137 offset:20480\n"
    "ds_read_b64_tr_b16 v[198:199], v141 offset:20480\n"
    "s_add_u32 m0, s10, 0x12400\n"
    "s_add_u32 s31, s35, 0x134000\n"
    "buffer_load_dwordx4 v13, s[44:47], s31 offen lds\n"
    "ds_read_b64_tr_b16 v[200:201], v138 offset:20480\n"
    "ds_read_b64_tr_b16 v[202:203], v142 offset:20480\n"
    "s_add_u32 m0, s10, 0x14000\n"
    "s_add_u32 s31, s36, 0x150000\n"
    "buffer_load_dwordx4 v13, s[44:47], s31 offen lds\n"
    "ds_read_b64_tr_b16 v[204:205], v139 offset:20480\n"
    "ds_read_b64_tr_b16 v[206:207], v143 offset:20480\n"
    "s_add_u32 m0, s10, 0x14400\n"
    "s_add_u32 s31, s36, 0x150000\n"
    "buffer_load_dwordx4 v14, s[44:47], s31 offen lds\n"
    "ds_read_b64_tr_b16 v[208:209], v136 offset:24576\n"
    "ds_read_b64_tr_b16 v[210:211], v140 offset:24576\n"
    "s_add_u32 m0, s10, 0x16000\n"
    "s_add_u32 s31, s37, 0x16c000\n"
    "buffer_load_dwordx4 v14, s[44:47], s31 offen lds\n"
    "ds_read_b64_tr_b16 v[212:213], v137 offset:24576\n"
    "ds_read_b64_tr_b16 v[214:215], v141 offset:24576\n"
    "s_add_u32 m0, s10, 0x16400\n"
    "s_add_u32 s31, s37, 0x16c000\n"
    "buffer_load_dwordx4 v15, s[44:47], s31 offen lds\n"
    "ds_read_b64_tr_b16 v[216:217], v138 offset:24576\n"
    "ds_read_b64_tr_b16 v[218:219], v142 offset:24576\n"
    "ds_read_b64_tr_b16 v[220:221], v139 offset:24576\n"
    "ds_read_b64_tr_b16 v[222:223], v143 offset:24576\n"
    "ds_read_b64_tr_b16 v[224:225], v136 offset:28672\n"
    "ds_read_b64_tr_b16 v[226:227], v140 offset:28672\n"
    "ds_read_b64_tr_b16 v[228:229], v137 offset:28672\n"
    "ds_read_b64_tr_b16 v[230:231], v141 offset:28672\n"
    "ds_read_b64_tr_b16 v[232:233], v138 offset:28672\n"
    "ds_read_b64_tr_b16 v[234:235], v142 offset:28672\n"
    "ds_read_b64_tr_b16 v[236:237], v139 offset:28672\n"
    "ds_read_b64_tr_b16 v[238:239], v143 offset:28672\n"
    "ds_read_b64_tr_b16 v[240:241], v136 offset:32768\n"
    "ds_read_b64_tr_b16 v[242:243], v140 offset:32768\n"
    "ds_read_b64_tr_b16 v[244:245], v137 offset:32768\n"
    "ds_read_b64_tr_b16 v[246:247], v141 offset:32768\n"
    "ds_read_b64_tr_b16 v[248:249], v138 offset:32768\n"
    "ds_read_b64_tr_b16 v[250:251], v142 offset:32768\n"
    "ds_read_b64_tr_b16 v[252:253], v139 offset:32768\n"
    "ds_read_b64_tr_b16 v[254:255], v143 offset:32768\n"
    "v_permlane16_swap_b32 v40, v42\n"
    "v_permlane16_swap_b32 v41, v43\n"
    "v_permlane16_swap_b32 v44, v46\n"
    "v_permlane16_swap_b32 v45, v47\n"
    "v_lshlrev_b32 v112, 16, v40\n"
    "v_and_b32 v113, 0xffff0000, v40\n"
    "v_lshlrev_b32 v114, 16, v41\n"
    "v_and_b32 v115, 0xffff0000, v41\n"
    "v_lshlrev_b32 v116, 16, v42\n"
    "v_and_b32 v117, 0xffff0000, v42\n"
    "v_lshlrev_b32 v118, 16, v43\n"
    "v_and_b32 v119, 0xffff0000, v43\n"
    "v_lshlrev_b32 v120, 16, v44\n"
    "v_and_b32 v121, 0xffff0000, v44\n"
    "v_lshlrev_b32 v122, 16, v45\n"
    "v_and_b32 v123, 0xffff0000, v45\n"
    "v_lshlrev_b32 v124, 16, v46\n"
    "v_and_b32 v125, 0xffff0000, v46\n"
    "v_lshlrev_b32 v126, 16, v47\n"
    "v_and_b32 v127, 0xffff0000, v47\n"
    "buffer_load_dwordx2 v[26:27], v18, s[56:59], s16 offen sc1\n"
    "s_lshr_b32 s40, s72, 5\n"
    "s_add_u32 s40, s40, s17\n"
    "s_lshr_b32 s41, s73, 5\n"
    "s_add_u32 s41, s41, s17\n"
    "s_lshr_b32 s42, s74, 5\n"
    "s_add_u32 s42, s42, s17\n"
    "s_lshr_b32 s43, s75, 5\n"
    "s_add_u32 s43, s43, s17\n"
    "v_add_u32 v4, 0x1c000, v4\n"
    "v_add_u32 v5, 0x1c000, v5\n"
    "v_add_u32 v6, 0x1c000, v6\n"
    "v_add_u32 v7, 0x1c000, v7\n"
    "s_waitcnt lgkmcnt(0)\n"
    "s_barrier\n"
    "s_mov_b32 s60, 0x8000\n"
    "s_waitcnt vmcnt(0)\n"
    "L1_%=:\n"
    "v_cmp_ne_u64 vcc, v[26:27], v[24:25]\n"
    "s_and_b32 s88, vcc_lo, 0xffff\n"
    "s_cbranch_scc0 L2_%=\n"
    "buffer_load_dwordx2 v[26:27], v18, s[56:59], s16 offen sc1\n"
    "s_sub_u32 s60, s60, 1\n"
    "s_waitcnt vmcnt(0)\n"
    "s_cbranch_scc0 L1_%=\n"
    "L2_%=:\n"
    "buffer_load_dwordx4 v[48:51], v1, s[56:59], s40 offen sc1\n"
    "buffer_load_dwordx4 v[52:55], v1, s[56:59], s40 offen offset:1024 sc1\n"
    "buffer_load_dwordx4 v[56:59], v1, s[56:59], s40 offen offset:2048 sc1\n"
    "buffer_load_dwordx4 v[60:63], v1, s[56:59], s40 offen offset:3072 sc1\n"
    "buffer_load_dwordx4 v[64:67], v1, s[56:59], s41 offen sc1\n"
    "buffer_load_dwordx4 v[68:71], v1, s[56:59], s41 offen offset:1024 sc1\n"
    "buffer_load_dwordx4 v[72:75], v1, s[56:59], s41 offen offset:2048 sc1\n"
    "buffer_load_dwordx4 v[76:79], v1, s[56:59], s41 offen offset:3072 sc1\n"
    "buffer_load_dwordx4 v[80:83], v1, s[56:59], s42 offen sc1\n"
    "buffer_load_dwordx4 v[84:87], v1, s[56:59], s42 offen offset:1024 sc1\n"
    "buffer_load_dwordx4 v[88:91], v1, s[56:59], s42 offen offset:2048 sc1\n"
    "buffer_load_dwordx4 v[92:95], v1, s[56:59], s42 offen offset:3072 sc1\n"
    "buffer_load_dwordx4 v[96:99], v1, s[56:59], s43 offen sc1\n"
    "buffer_load_dwordx4 v[100:103], v1, s[56:59], s43 offen offset:1024 sc1\n"
    "buffer_load_dwordx4 v[104:107], v1, s[56:59], s43 offen offset:2048 sc1\n"
    "buffer_load_dwordx4 v[108:111], v1, s[56:59], s43 offen offset:3072 sc1\n"
    "s_waitcnt vmcnt(15)\n"
    "v_mfma_f32_16x16x32_bf16 v[112:115], a[96:99], v[48:51], v[112:115]\n"
    "v_mfma_f32_16x16x32_bf16 v[116:119], a[100:103], v[48:51], v[116:119]\n"
    "v_mfma_f32_16x16x32_bf16 v[120:123], a[104:107], v[48:51], v[120:123]\n"
    "v_mfma_f32_16x16x32_bf16 v[124:127], a[108:111], v[48:51], v[124:127]\n"
    "s_waitcnt vmcnt(14)\n"
    "v_mfma_f32_16x16x32_bf16 v[112:115], a[112:115], v[52:55], v[112:115]\n"
    "v_mfma_f32_16x16x32_bf16 v[116:119], a[116:119], v[52:55], v[116:119]\n"
    "v_mfma_f32_16x16x32_bf16 v[120:123], a[120:123], v[52:55], v[120:123]\n"
    "v_mfma_f32_16x16x32_bf16 v[124:127], a[124:127], v[52:55], v[124:127]\n"
    "s_waitcnt vmcnt(13)\n"
    "v_mfma_f32_16x16x32_bf16 v[112:115], a[128:131], v[56:59], v[112:115]\n"
    "v_mfma_f32_16x16x32_bf16 v[116:119], a[132:135], v[56:59], v[116:119]\n"
    "v_mfma_f32_16x16x32_bf16 v[120:123], a[136:139], v[56:59], v[120:123]\n"
    "v_mfma_f32_16x16x32_bf16 v[124:127], a[140:143], v[56:59], v[124:127]\n"
    "s_waitcnt vmcnt(12)\n"
    "v_mfma_f32_16x16x32_bf16 v[112:115], a[144:147], v[60:63], v[112:115]\n"
    "v_mfma_f32_16x16x32_bf16 v[116:119], a[148:151], v[60:63], v[116:119]\n"
    "v_mfma_f32_16x16x32_bf16 v[120:123], a[152:155], v[60:63], v[120:123]\n"
    "v_mfma_f32_16x16x32_bf16 v[124:127], a[156:159], v[60:63], v[124:127]\n"
    "s_waitcnt vmcnt(11)\n"
    "v_mfma_f32_16x16x32_bf16 v[112:115], a[160:163], v[64:67], v[112:115]\n"
    "v_mfma_f32_16x16x32_bf16 v[116:119], a[164:167], v[64:67], v[116:119]\n"
    "v_mfma_f32_16x16x32_bf16 v[120:123], a[168:171], v[64:67], v[120:123]\n"
    "v_mfma_f32_16x16x32_bf16 v[124:127], a[172:175], v[64:67], v[124:127]\n"
    "s_waitcnt vmcnt(10)\n"
    "v_mfma_f32_16x16x32_bf16 v[112:115], a[176:179], v[68:71], v[112:115]\n"
    "v_mfma_f32_16x16x32_bf16 v[116:119], a[180:183], v[68:71], v[116:119]\n"
    "v_mfma_f32_16x16x32_bf16 v[120:123], a[184:187], v[68:71], v[120:123]\n"
    "v_mfma_f32_16x16x32_bf16 v[124:127], a[188:191], v[68:71], v[124:127]\n"
    "s_waitcnt vmcnt(9)\n"
    "v_mfma_f32_16x16x32_bf16 v[112:115], a[192:195], v[72:75], v[112:115]\n"
    "v_mfma_f32_16x16x32_bf16 v[116:119], a[196:199], v[72:75], v[116:119]\n"
    "v_mfma_f32_16x16x32_bf16 v[120:123], a[200:203], v[72:75], v[120:123]\n"
    "v_mfma_f32_16x16x32_bf16 v[124:127], a[204:207], v[72:75], v[124:127]\n"
    "s_waitcnt vmcnt(8)\n"
    "v_mfma_f32_16x16x32_bf16 v[112:115], a[208:211], v[76:79], v[112:115]\n"
    "v_mfma_f32_16x16x32_bf16 v[116:119], a[212:215], v[76:79], v[116:119]\n"
    "v_mfma_f32_16x16x32_bf16 v[120:123], a[216:219], v[76:79], v[120:123]\n"
    "v_mfma_f32_16x16x32_bf16 v[124:127], a[220:223], v[76:79], v[124:127]\n"
    "s_waitcnt vmcnt(7)\n"
    "v_mfma_f32_16x16x32_bf16 v[112:115], a[224:227], v[80:83], v[112:115]\n"
    "v_mfma_f32_16x16x32_bf16 v[116:119], a[228:231], v[80:83], v[116:119]\n"
    "v_mfma_f32_16x16x32_bf16 v[120:123], a[232:235], v[80:83], v[120:123]\n"
    "v_mfma_f32_16x16x32_bf16 v[124:127], a[236:239], v[80:83], v[124:127]\n"
    "s_waitcnt vmcnt(6)\n"
    "v_mfma_f32_16x16x32_bf16 v[112:115], a[240:243], v[84:87], v[112:115]\n"
    "v_mfma_f32_16x16x32_bf16 v[116:119], a[244:247], v[84:87], v[116:119]\n"
    "v_mfma_f32_16x16x32_bf16 v[120:123], a[248:251], v[84:87], v[120:123]\n"
    "v_mfma_f32_16x16x32_bf16 v[124:127], a[252:255], v[84:87], v[124:127]\n"
    "s_waitcnt vmcnt(5)\n"
    "v_mfma_f32_16x16x32_bf16 v[112:115], v[160:163], v[88:91], v[112:115]\n"
    "v_mfma_f32_16x16x32_bf16 v[116:119], v[164:167], v[88:91], v[116:119]\n"
    "s_add_u32 m0, s10, 0x18000\n"
    "s_add_u32 s31, s37, 0x188000\n"
    "buffer_load_dwordx4 v15, s[44:47], s31 offen lds\n"
    "v_mfma_f32_16x16x32_bf16 v[120:123], v[168:171], v[88:91], v[120:123]\n"
    "v_mfma_f32_16x16x32_bf16 v[124:127], v[172:175], v[88:91], v[124:127]\n"
    "s_waitcnt vmcnt(5)\n"
    "v_mfma_f32_16x16x32_bf16 v[112:115], v[176:179], v[92:95], v[112:115]\n"
    "v_mfma_f32_16x16x32_bf16 v[116:119], v[180:183], v[92:95], v[116:119]\n"
    "s_add_u32 m0, s10, 0x18400\n"
    "s_add_u32 s31, s37, 0x188000\n"
    "buffer_load_dwordx4 v16, s[44:47], s31 offen lds\n"
    "v_mfma_f32_16x16x32_bf16 v[120:123], v[184:187], v[92:95], v[120:123]\n"
    "v_mfma_f32_16x16x32_bf16 v[124:127], v[188:191], v[92:95], v[124:127]\n"
    "s_waitcnt vmcnt(5)\n"
    "v_mfma_f32_16x16x32_bf16 v[112:115], v[192:195], v[96:99], v[112:115]\n"
    "v_mfma_f32_16x16x32_bf16 v[116:119], v[196:199], v[96:99], v[116:119]\n"
    "s_add_u32 m0, s10, 0x1a000\n"
    "s_add_u32 s31, s37, 0x1a4000\n"
    "buffer_load_dwordx4 v16, s[44:47], s31 offen lds\n"
    "v_mfma_f32_16x16x32_bf16 v[120:123], v[200:203], v[96:99], v[120:123]\n"
    "v_mfma_f32_16x16x32_bf16 v[124:127], v[204:207], v[96:99], v[124:127]\n"
    "s_waitcnt vmcnt(5)\n"
    "v_mfma_f32_16x16x32_bf16 v[112:115], v[208:211], v[100:103], v[112:115]\n"
    "v_mfma_f32_16x16x32_bf16 v[116:119], v[212:215], v[100:103], v[116:119]\n"
    "s_add_u32 m0, s10, 0x1a400\n"
    "s_add_u32 s31, s37, 0x1c0000\n"
    "buffer_load_dwordx4 v10, s[44:47], s31 offen lds\n"
    "v_mfma_f32_16x16x32_bf16 v[120:123], v[216:219], v[100:103], v[120:123]\n"
    "v_mfma_f32_16x16x32_bf16 v[124:127], v[220:223], v[100:103], v[124:127]\n"
    "s_waitcnt vmcnt(5)\n"
    "v_mfma_f32_16x16x32_bf16 v[112:115], v[224:227], v[104:107], v[112:115]\n"
    "v_mfma_f32_16x16x32_bf16 v[116:119], v[228:231], v[104:107], v[116:119]\n"
    "v_mfma_f32_16x16x32_bf16 v[120:123], v[232:235], v[104:107], v[120:123]\n"
    "v_mfma_f32_16x16x32_bf16 v[124:127], v[236:239], v[104:107], v[124:127]\n"
    "s_waitcnt vmcnt(4)\n"
    "v_mfma_f32_16x16x32_bf16 v[112:115], v[240:243], v[108:111], v[112:115]\n"
    "v_mfma_f32_16x16x32_bf16 v[116:119], v[244:247], v[108:111], v[116:119]\n"
    "v_mfma_f32_16x16x32_bf16 v[120:123], v[248:251], v[108:111], v[120:123]\n"
    "v_mfma_f32_16x16x32_bf16 v[124:127], v[252:255], v[108:111], v[124:127]\n"
    "s_nop 4\n"
    "v_cvt_pk_bf16_f32 v40, v112, v113\n"
    "v_cvt_pk_bf16_f32 v41, v114, v115\n"
    "v_cvt_pk_bf16_f32 v42, v116, v117\n"
    "v_cvt_pk_bf16_f32 v43, v118, v119\n"
    "v_cvt_pk_bf16_f32 v44, v120, v121\n"
    "v_cvt_pk_bf16_f32 v45, v122, v123\n"
    "v_cvt_pk_bf16_f32 v46, v124, v125\n"
    "v_cvt_pk_bf16_f32 v47, v126, v127\n"
    "buffer_store_dwordx4 v[40:43], v1, s[56:59], s18 offen sc1\n"
    "buffer_store_dwordx4 v[44:47], v1, s[56:59], s18 offen offset:1024 sc1\n"
    "v_mfma_f32_16x16x32_bf16 v[132:135], v[40:43], v[40:43], 0\n"
    "v_mfma_f32_16x16x32_bf16 v[128:131], v[28:31], v[40:43], 0\n"
    "v_mfma_f32_16x16x32_bf16 v[132:135], v[44:47], v[44:47], v[132:135]\n"
    "v_mfma_f32_16x16x32_bf16 v[128:131], v[28:31], v[44:47], v[128:131]\n"
    "s_nop 6\n"
    "v_cndmask_b32_e64 v132, v132, v133, s[62:63]\n"
    "v_cndmask_b32_e64 v134, v134, v135, s[62:63]\n"
    "v_cndmask_b32_e64 v129, v132, v134, s[64:65]\n"
    "buffer_store_dwordx2 v[128:129], v20, s[56:59], s19 offen sc1\n"
    "s_add_u32 m0, s10, 0\n"
    "s_add_u32 s31, s32, 0x1c000\n"
    "buffer_load_dwordx4 v10, s[44:47], s31 offen lds\n"
    "s_add_u32 m0, s10, 0x400\n"
    "s_add_u32 s31, s32, 0x1c000\n"
    "buffer_load_dwordx4 v11, s[44:47], s31 offen lds\n"
    "s_add_u32 m0, s10, 0x2000\n"
    "s_add_u32 s31, s32, 0x38000\n"
    "buffer_load_dwordx4 v11, s[44:47], s31 offen lds\n"
    "s_add_u32 m0, s10, 0x2400\n"
    "s_add_u32 s31, s32, 0x38000\n"
    "buffer_load_dwordx4 v12, s[44:47], s31 offen lds\n"
    "s_add_u32 m0, s10, 0x4000\n"
    "s_add_u32 s31, s32, 0x54000\n"
    "buffer_load_dwordx4 v12, s[44:47], s31 offen lds\n"
    "s_add_u32 m0, s10, 0x4400\n"
    "s_add_u32 s31, s32, 0x54000\n"
    "buffer_load_dwordx4 v13, s[44:47], s31 offen lds\n"
    "s_add_u32 m0, s10, 0x6000\n"
    "s_add_u32 s31, s33, 0x70000\n"
    "buffer_load_dwordx4 v13, s[44:47], s31 offen lds\n"
    "s_add_u32 m0, s10, 0x6400\n"
    "s_add_u32 s31, s33, 0x70000\n"
    "buffer_load_dwordx4 v14, s[44:47], s31 offen lds\n"
    "s_waitcnt vmcnt(8)\n"
    "buffer_store_dwordx2 v[24:25], v17, s[56:59], s20 offen sc1\n"
    "s_add_u32 m0, s10, 0x8000\n"
    "s_add_u32 s31, s34, 0x8c000\n"
    "buffer_load_dwordx4 v14, s[44:47], s31 offen lds\n"
    "s_add_u32 m0, s10, 0x8400\n"
    "s_add_u32 s31, s34, 0x8c000\n"
    "buffer_load_dwordx4 v15, s[44:47], s31 offen lds\n"
    "s_add_u32 m0, s10, 0x1c000\n"
    "s_add_u32 s31, s76, 0\n"
    "buffer_load_dwordx4 v2, s[48:51], s31 offen lds\n"
    "buffer_load_dwordx4 v3, s[48:51], s31 offen offset:1024 lds\n"
    "s_add_u32 m0, s10, 0x1e000\n"
    "s_add_u32 s31, s76, 0x4000\n"
    "buffer_load_dwordx4 v2, s[48:51], s31 offen lds\n"
    "buffer_load_dwordx4 v3, s[48:51], s31 offen offset:1024 lds\n"
    "s_add_u32 m0, s10, 0x20000\n"
    "s_add_u32 s31, s84, 0x18000\n"
    "buffer_load_dwordx4 v2, s[48:51], s31 offen lds\n"
    "buffer_load_dwordx4 v3, s[48:51], s31 offen offset:1024 lds\n"
    "s_add_u32 m0, s10, 0x22000\n"
    "s_add_u32 s31, s84, 0x34000\n"
    "buffer_load_dwordx4 v2, s[48:51], s31 offen lds\n"
    "buffer_load_dwordx4 v3, s[48:51], s31 offen offset:1024 lds\n"
    "s_add_u32 m0, s10, 0x24000\n"
    "s_add_u32 s31, s76, 0x8000\n"
    "buffer_load_dwordx4 v2, s[48:51], s31 offen lds\n"
    "buffer_load_dwordx4 v3, s[48:51], s31 offen offset:1024 lds\n"
    "v_lshlrev_b32 v112, 16, v40\n"
    "v_and_b32 v113, 0xffff0000, v40\n"
    "v_lshlrev_b32 v114, 16, v41\n"
    "v_and_b32 v115, 0xffff0000, v41\n"
    "v_lshlrev_b32 v116, 16, v42\n"
    "v_and_b32 v117, 0xffff0000, v42\n"
    "v_lshlrev_b32 v118, 16, v43\n"
    "v_and_b32 v119, 0xffff0000, v43\n"
    "v_lshlrev_b32 v120, 16, v44\n"
    "v_and_b32 v121, 0xffff0000, v44\n"
    "v_lshlrev_b32 v122, 16, v45\n"
    "v_and_b32 v123, 0xffff0000, v45\n"
    "v_lshlrev_b32 v124, 16, v46\n"
    "v_and_b32 v125, 0xffff0000, v46\n"
    "v_lshlrev_b32 v126, 16, v47\n"
    "v_and_b32 v127, 0xffff0000, v47\n"
    "s_xor_b32 s28, s8, 1\n"
    "s_mul_i32 s28, s28, 0x3000\n"
    "s_add_u32 s28, s28, 0\n"
    "v_add_u32 v41, s28, v1\n"
    "s_xor_b32 s28, s8, 2\n"
    "s_mul_i32 s28, s28, 0x3000\n"
    "s_add_u32 s28, s28, 0x1000\n"
    "v_add_u32 v42, s28, v1\n"
    "s_xor_b32 s28, s8, 3\n"
    "s_mul_i32 s28, s28, 0x3000\n"
    "s_add_u32 s28, s28, 0x2000\n"
    "v_add_u32 v43, s28, v1\n"
    "s_mul_i32 s28, s8, 0x3000\n"
    "v_add_u32 v40, s28, v1\n"
    "s_add_u32 s28, s90, 0\n"
    "s_and_b32 s28, s28, 3\n"
    "s_lshl_b32 s28, s28, 12\n"
    "s_add_u32 s32, s28, s22\n"
    "s_add_u32 s36, s32, 0x4000\n"
    "s_add_u32 s28, s90, 1\n"
    "s_and_b32 s28, s28, 3\n"
    "s_lshl_b32 s28, s28, 12\n"
    "s_add_u32 s33, s28, s22\n"
    "s_add_u32 s37, s33, 0x4000\n"
    "s_add_u32 s28, s90, 2\n"
    "s_and_b32 s28, s28, 3\n"
    "s_lshl_b32 s28, s28, 12\n"
    "s_add_u32 s34, s28, s22\n"
    "s_add_u32 s38, s34, 0x4000\n"
    "s_add_u32 s28, s90, 3\n"
    "s_and_b32 s28, s28, 3\n"
    "s_lshl_b32 s28, s28, 12\n"
    "s_add_u32 s35, s28, s22\n"
    "s_add_u32 s39, s35, 0x4000\n"
    "s_lshl_b32 s28, s8, 5\n"
    "s_add_u32 s29, s8, 4\n"
    "s_min_u32 s29, s29, 6\n"
    "s_lshl_b32 s29, s29, 5\n"
    "v_add_u32 v144, s28, v8\n"
    "v_add_u32 v146, s29, v8\n"
    "v_add_u32 v145, s28, v9\n"
    "v_add_u32 v147, s29, v9\n"
    "s_lshl_b32 s28, s28, 1\n"
    "s_lshl_b32 s29, s29, 1\n"
    "v_add_u32 v36, s28, v23\n"
    "v_add_u32 v37, s29, v23\n"
    "s_waitcnt vmcnt(10)\n"
    "s_barrier\n"
    "ds_read_b64_tr_b16 v[48:49], v144\n"
    "ds_read_b64_tr_b16 v[50:51], v144 offset:3584\n"
    "ds_read_b64_tr_b16 v[52:53], v146\n"
    "ds_read_b64_tr_b16 v[54:55], v146 offset:3584\n"
    "ds_read_b64_tr_b16 v[56:57], v144 offset:7168\n"
    "ds_read_b64_tr_b16 v[58:59], v144 offset:10752\n"
    "ds_read_b64_tr_b16 v[60:61], v146 offset:7168\n"
    "ds_read_b64_tr_b16 v[62:63], v146 offset:10752\n"
    "ds_read_b64_tr_b16 v[64:65], v144 offset:14336\n"
    "ds_read_b64_tr_b16 v[66:67], v144 offset:17920\n"
    "ds_read_b64_tr_b16 v[68:69], v146 offset:14336\n"
    "ds_read_b64_tr_b16 v[70:71], v146 offset:17920\n"
    "ds_read_b64_tr_b16 v[72:73], v144 offset:21504\n"
    "ds_read_b64_tr_b16 v[74:75], v144 offset:25088\n"
    "ds_read_b64_tr_b16 v[76:77], v146 offset:21504\n"
    "ds_read_b64_tr_b16 v[78:79], v146 offset:25088\n"
    "s_waitcnt lgkmcnt(12)\n"
    "v_mfma_f32_16x16x32_bf16 v[136:139], v[48:51], v[28:31], 0\n"
    "v_mfma_f32_16x16x32_bf16 v[140:143], v[52:55], v[28:31], 0\n"
    "ds_read_b64_tr_b16 v[48:49], v144 offset:28672\n"
    "ds_read_b64_tr_b16 v[50:51], v144 offset:32256\n"
    "ds_read_b64_tr_b16 v[52:53], v146 offset:28672\n"
    "ds_read_b64_tr_b16 v[54:55], v146 offset:32256\n"
    "s_waitcnt lgkmcnt(12)\n"
    "v_mfma_f32_16x16x32_bf16 v[136:139], v[56:59], v[28:31], v[136:139]\n"
    "v_mfma_f32_16x16x32_bf16 v[140:143], v[60:63], v[28:31], v[140:143]\n"
    "ds_read_b64_tr_b16 v[56:57], v144 offset:35840\n"
    "ds_read_b64_tr_b16 v[58:59], v144 offset:39424\n"
    "ds_read_b64_tr_b16 v[60:61], v146 offset:35840\n"
    "ds_read_b64_tr_b16 v[62:63], v146 offset:39424\n"
    "s_waitcnt lgkmcnt(12)\n"
    "v_mfma_f32_16x16x32_bf16 v[136:139], v[64:67], v[28:31], v[136:139]\n"
    "v_mfma_f32_16x16x32_bf16 v[140:143], v[68:71], v[28:31], v[140:143]\n"
    "ds_read_b64_tr_b16 v[64:65], v144 offset:43008\n"
    "ds_read_b64_tr_b16 v[66:67], v144 offset:46592\n"
    "ds_read_b64_tr_b16 v[68:69], v146 offset:43008\n"
    "ds_read_b64_tr_b16 v[70:71], v146 offset:46592\n"
    "s_waitcnt lgkmcnt(12)\n"
    "v_mfma_f32_16x16x32_bf16 v[136:139], v[72:75], v[28:31], v[136:139]\n"
    "v_mfma_f32_16x16x32_bf16 v[140:143], v[76:79], v[28:31], v[140:143]\n"
    "ds_read_b64_tr_b16 v[72:73], v144 offset:50176\n"
    "ds_read_b64_tr_b16 v[74:75], v144 offset:53760\n"
    "ds_read_b64_tr_b16 v[76:77], v146 offset:50176\n"
    "ds_read_b64_tr_b16 v[78:79], v146 offset:53760\n"
    "s_waitcnt lgkmcnt(12)\n"
    "v_mfma_f32_16x16x32_bf16 v[136:139], v[48:51], v[28:31], v[136:139]\n"
    "v_mfma_f32_16x16x32_bf16 v[140:143], v[52:55], v[28:31], v[140:143]\n"
    "ds_read_b64_tr_b16 v[48:49], v145\n"
    "ds_read_b64_tr_b16 v[50:51], v145 offset:3584\n"
    "ds_read_b64_tr_b16 v[52:53], v147\n"
    "ds_read_b64_tr_b16 v[54:55], v147 offset:3584\n"
    "s_waitcnt lgkmcnt(12)\n"
    "v_mfma_f32_16x16x32_bf16 v[136:139], v[56:59], v[28:31], v[136:139]\n"
    "v_mfma_f32_16x16x32_bf16 v[140:143], v[60:63], v[28:31], v[140:143]\n"
    "ds_read_b64_tr_b16 v[56:57], v145 offset:7168\n"
    "ds_read_b64_tr_b16 v[58:59], v145 offset:10752\n"
    "ds_read_b64_tr_b16 v[60:61], v147 offset:7168\n"
    "ds_read_b64_tr_b16 v[62:63], v147 offset:10752\n"
    "s_waitcnt lgkmcnt(12)\n"
    "v_mfma_f32_16x16x32_bf16 v[136:139], v[64:67], v[28:31], v[136:139]\n"
    "v_mfma_f32_16x16x32_bf16 v[140:143], v[68:71], v[28:31], v[140:143]\n"
    "ds_read_b64_tr_b16 v[64:65], v145 offset:14336\n"
    "ds_read_b64_tr_b16 v[66:67], v145 offset:17920\n"
    "ds_read_b64_tr_b16 v[68:69], v147 offset:14336\n"
    "ds_read_b64_tr_b16 v[70:71], v147 offset:17920\n"
    "s_waitcnt lgkmcnt(12)\n"
    "v_mfma_f32_16x16x32_bf16 v[136:139], v[72:75], v[28:31], v[136:139]\n"
    "v_mfma_f32_16x16x32_bf16 v[140:143], v[76:79], v[28:31], v[140:143]\n"
    "buffer_load_dwordx2 v[26:27], v18, s[56:59], s21 offen sc1\n"
    "ds_read_b64_tr_b16 v[72:73], v145 offset:21504\n"
    "ds_read_b64_tr_b16 v[74:75], v145 offset:25088\n"
    "ds_read_b64_tr_b16 v[76:77], v147 offset:21504\n"
    "ds_read_b64_tr_b16 v[78:79], v147 offset:25088\n"
    "s_waitcnt lgkmcnt(12)\n"
    "v_mfma_f32_16x16x32_bf16 v[136:139], v[48:51], v[28:31], v[136:139]\n"
    "v_mfma_f32_16x16x32_bf16 v[140:143], v[52:55], v[28:31], v[140:143]\n"
    "ds_read_b64_tr_b16 v[48:49], v145 offset:28672\n"
    "ds_read_b64_tr_b16 v[50:51], v145 offset:32256\n"
    "ds_read_b64_tr_b16 v[52:53], v147 offset:28672\n"
    "ds_read_b64_tr_b16 v[54:55], v147 offset:32256\n"
    "s_waitcnt lgkmcnt(12)\n"
    "v_mfma_f32_16x16x32_bf16 v[136:139], v[56:59], v[28:31], v[136:139]\n"
    "v_mfma_f32_16x16x32_bf16 v[140:143], v[60:63], v[28:31], v[140:143]\n"
    "ds_read_b64_tr_b16 v[56:57], v145 offset:35840\n"
    "ds_read_b64_tr_b16 v[58:59], v145 offset:39424\n"
    "ds_read_b64_tr_b16 v[60:61], v147 offset:35840\n"
    "ds_read_b64_tr_b16 v[62:63], v147 offset:39424\n"
    "s_waitcnt lgkmcnt(12)\n"
    "v_mfma_f32_16x16x32_bf16 v[136:139], v[64:67], v[28:31], v[136:139]\n"
    "v_mfma_f32_16x16x32_bf16 v[140:143], v[68:71], v[28:31], v[140:143]\n"
    "ds_read_b64_tr_b16 v[64:65], v145 offset:43008\n"
    "ds_read_b64_tr_b16 v[66:67], v145 offset:46592\n"
    "ds_read_b64_tr_b16 v[68:69], v147 offset:43008\n"
    "ds_read_b64_tr_b16 v[70:71], v147 offset:46592\n"
    "s_waitcnt lgkmcnt(12)\n"
    "v_mfma_f32_16x16x32_bf16 v[136:139], v[72:75], v[28:31], v[136:139]\n"
    "v_mfma_f32_16x16x32_bf16 v[140:143], v[76:79], v[28:31], v[140:143]\n"
    "ds_read_b64_tr_b16 v[72:73], v145 offset:50176\n"
    "ds_read_b64_tr_b16 v[74:75], v145 offset:53760\n"
    "ds_read_b64_tr_b16 v[76:77], v147 offset:50176\n"
    "ds_read_b64_tr_b16 v[78:79], v147 offset:53760\n"
    "s_waitcnt lgkmcnt(12)\n"
    "v_mfma_f32_16x16x32_bf16 v[136:139], v[48:51], v[28:31], v[136:139]\n"
    "v_mfma_f32_16x16x32_bf16 v[140:143], v[52:55], v[28:31], v[140:143]\n"
    "s_waitcnt lgkmcnt(8)\n"
    "v_mfma_f32_16x16x32_bf16 v[136:139], v[56:59], v[28:31], v[136:139]\n"
    "v_mfma_f32_16x16x32_bf16 v[140:143], v[60:63], v[28:31], v[140:143]\n"
    "s_waitcnt lgkmcnt(4)\n"
    "v_mfma_f32_16x16x32_bf16 v[136:139], v[64:67], v[28:31], v[136:139]\n"
    "v_mfma_f32_16x16x32_bf16 v[140:143], v[68:71], v[28:31], v[140:143]\n"
    "s_waitcnt lgkmcnt(0)\n"
    "v_mfma_f32_16x16x32_bf16 v[136:139], v[72:75], v[28:31], v[136:139]\n"
    "v_mfma_f32_16x16x32_bf16 v[140:143], v[76:79], v[28:31], v[140:143]\n"
    "s_nop 6\n"
    "ds_write_b128 v36, v[136:139]\n"
    "ds_write_b128 v37, v[140:143]\n"
    "ds_read_b64_tr_b16 a[200:201], v8\n"
    "ds_read_b64_tr_b16 a[202:203], v8 offset:3584\n"
    "ds_read_b64_tr_b16 a[204:205], v8 offset:32\n"
    "ds_read_b64_tr_b16 a[206:207], v8 offset:3616\n"
    "ds_read_b64_tr_b16 a[208:209], v8 offset:64\n"
    "ds_read_b64_tr_b16 a[210:211], v8 offset:3648\n"
    "ds_read_b64_tr_b16 a[212:213], v8 offset:96\n"
    "ds_read_b64_tr_b16 a[214:215], v8 offset:3680\n"
    "ds_read_b64_tr_b16 a[216:217], v8 offset:128\n"
    "ds_read_b64_tr_b16 a[218:219], v8 offset:3712\n"
    "ds_read_b64_tr_b16 a[220:221], v8 offset:160\n"
    "ds_read_b64_tr_b16 a[222:223], v8 offset:3744\n"
    "ds_read_b64_tr_b16 a[224:225], v8 offset:192\n"
    "ds_read_b64_tr_b16 a[226:227], v8 offset:3776\n"
    "s_mov_b32 s60, 0x8000\n"
    "s_waitcnt vmcnt(0)\n"
    "L3_%=:\n"
    "v_cmp_ne_u64 vcc, v[26:27], v[24:25]\n"
    "s_and_b32 s88, vcc_lo, 0xffff\n"
    "s_cbranch_scc0 L4_%=\n"
    "buffer_load_dwordx2 v[26:27], v18, s[56:59], s21 offen sc1\n"
    "s_sub_u32 s60, s60, 1\n"
    "s_waitcnt vmcnt(0)\n"
    "s_cbranch_scc0 L3_%=\n"
    "L4_%=:\n"
    "buffer_load_dwordx4 v[48:51], v1, s[56:59], s32 offen sc1\n"
    "buffer_load_dwordx4 v[52:55], v1, s[56:59], s36 offen sc1\n"
    "buffer_load_dwordx4 v[56:59], v1, s[56:59], s32 offen offset:1024 sc1\n"
    "buffer_load_dwordx4 v[60:63], v1, s[56:59], s36 offen offset:1024 sc1\n"
    "buffer_load_dwordx4 v[64:67], v1, s[56:59], s32 offen offset:2048 sc1\n"
    "buffer_load_dwordx4 v[68:71], v1, s[56:59], s36 offen offset:2048 sc1\n"
    "buffer_load_dwordx4 v[72:75], v1, s[56:59], s32 offen offset:3072 sc1\n"
    "buffer_load_dwordx4 v[76:79], v1, s[56:59], s36 offen offset:3072 sc1\n"
    "buffer_load_dwordx4 v[80:83], v1, s[56:59], s33 offen sc1\n"
    "buffer_load_dwordx4 v[84:87], v1, s[56:59], s37 offen sc1\n"
    "ds_read_b64_tr_b16 a[0:1], v4\n"
    "ds_read_b64_tr_b16 a[4:5], v5\n"
    "ds_read_b64_tr_b16 a[8:9], v6\n"
    "ds_read_b64_tr_b16 a[12:13], v7\n"
    "ds_read_b64_tr_b16 a[2:3], v4 offset:8192\n"
    "ds_read_b64_tr_b16 a[6:7], v5 offset:8192\n"
    "ds_read_b64_tr_b16 a[10:11], v6 offset:8192\n"
    "ds_read_b64_tr_b16 a[14:15], v7 offset:8192\n"
    "ds_read_b64_tr_b16 a[64:65], v4 offset:16384\n"
    "ds_read_b64_tr_b16 a[68:69], v5 offset:16384\n"
    "ds_read_b64_tr_b16 a[72:73], v6 offset:16384\n"
    "ds_read_b64_tr_b16 a[76:77], v7 offset:16384\n"
    "ds_read_b64_tr_b16 a[66:67], v4 offset:24576\n"
    "ds_read_b64_tr_b16 a[70:71], v5 offset:24576\n"
    "ds_read_b64_tr_b16 a[74:75], v6 offset:24576\n"
    "ds_read_b64_tr_b16 a[78:79], v7 offset:24576\n"
    "ds_read_b64_tr_b16 a[96:97], v4 offset:32768\n"
    "ds_read_b64_tr_b16 a[100:101], v5 offset:32768\n"
    "ds_read_b64_tr_b16 a[104:105], v6 offset:32768\n"
    "ds_read_b64_tr_b16 a[108:109], v7 offset:32768\n"
    "s_add_u32 s91, s24, 0x1000\n"
    "s_waitcnt lgkmcnt(15)\n"
    "s_waitcnt vmcnt(8)\n"
    "v_mfma_f32_16x16x32_bf16 v[176:179], a[200:203], v[48:51], 0\n"
    "ds_read_b64_tr_b16 a[228:229], v8 offset:7168\n"
    "v_mfma_f32_16x16x32_bf16 v[204:207], a[200:203], v[52:55], 0\n"
    "ds_read_b64_tr_b16 a[230:231], v8 offset:10752\n"
    "buffer_load_dwordx4 v[88:91], v1, s[56:59], s33 offen offset:1024 sc1\n"
    "v_mfma_f32_16x16x32_bf16 v[180:183], a[204:207], v[48:51], 0\n"
    "ds_read_b64_tr_b16 a[232:233], v8 offset:7200\n"
    "v_mfma_f32_16x16x32_bf16 v[208:211], a[204:207], v[52:55], 0\n"
    "ds_read_b64_tr_b16 a[234:235], v8 offset:10784\n"
    "buffer_load_dwordx4 v[92:95], v1, s[56:59], s37 offen offset:1024 sc1\n"
    "v_mfma_f32_16x16x32_bf16 v[184:187], a[208:211], v[48:51], 0\n"
    "ds_read_b64_tr_b16 a[236:237], v8 offset:7232\n"
    "v_mfma_f32_16x16x32_bf16 v[212:215], a[208:211], v[52:55], 0\n"
    "ds_read_b64_tr_b16 a[238:239], v8 offset:10816\n"
    "v_mfma_f32_16x16x32_bf16 v[188:191], a[212:215], v[48:51], 0\n"
    "ds_read_b64_tr_b16 a[240:241], v8 offset:7264\n"
    "v_mfma_f32_16x16x32_bf16 v[216:219], a[212:215], v[52:55], 0\n"
    "ds_read_b64_tr_b16 a[242:243], v8 offset:10848\n"
    "v_mfma_f32_16x16x32_bf16 v[192:195], a[216:219], v[48:51], 0\n"
    "ds_read_b64_tr_b16 a[244:245], v8 offset:7296\n"
    "v_mfma_f32_16x16x32_bf16 v[220:223], a[216:219], v[52:55], 0\n"
    "ds_read_b64_tr_b16 a[246:247], v8 offset:10880\n"
    "buffer_load_dwordx4 v[96:99], v1, s[56:59], s33 offen offset:2048 sc1\n"
    "v_mfma_f32_16x16x32_bf16 v[196:199], a[220:223], v[48:51], 0\n"
    "ds_read_b64_tr_b16 a[248:249], v8 offset:7328\n"
    "v_mfma_f32_16x16x32_bf16 v[224:227], a[220:223], v[52:55], 0\n"
    "ds_read_b64_tr_b16 a[250:251], v8 offset:10912\n"
    "buffer_load_dwordx4 v[100:103], v1, s[56:59], s37 offen offset:2048 sc1\n"
    "v_mfma_f32_16x16x32_bf16 v[200:203], a[224:227], v[48:51], 0\n"
    "ds_read_b64_tr_b16 a[252:253], v8 offset:7360\n"
    "v_mfma_f32_16x16x32_bf16 v[228:231], a[224:227], v[52:55], 0\n"
    "ds_read_b64_tr_b16 a[254:255], v8 offset:10944\n"
    "s_waitcnt lgkmcnt(6)\n"
    "s_waitcnt vmcnt(10)\n"
    "v_mfma_f32_16x16x32_bf16 v[176:179], a[228:231], v[56:59], v[176:179]\n"
    "ds_read_b64_tr_b16 a[200:201], v8 offset:14336\n"
    "v_mfma_f32_16x16x32_bf16 v[204:207], a[228:231], v[60:63], v[204:207]\n"
    "ds_read_b64_tr_b16 a[202:203], v8 offset:17920\n"
    "buffer_load_dwordx2 v[240:241], v21, s[56:59], s23 offen sc1\n"
    "buffer_load_dwordx2 v[242:243], v21, s[56:59], s23 offen offset:128 sc1\n"
    "buffer_load_dwordx2 v[244:245], v21, s[56:59], s23 offen offset:1024 sc1\n"
    "buffer_load_dwordx2 v[246:247], v21, s[56:59], s23 offen offset:1152 sc1\n"
    "v_mfma_f32_16x16x32_bf16 v[180:183], a[232:235], v[56:59], v[180:183]\n"
    "ds_read_b64_tr_b16 a[204:205], v8 offset:14368\n"
    "v_mfma_f32_16x16x32_bf16 v[208:211], a[232:235], v[60:63], v[208:211]\n"
    "ds_read_b64_tr_b16 a[206:207], v8 offset:17952\n"
    "buffer_load_dwordx4 v[104:107], v1, s[56:59], s33 offen offset:3072 sc1\n"
    "v_mfma_f32_16x16x32_bf16 v[184:187], a[236:239], v[56:59], v[184:187]\n"
    "ds_read_b64_tr_b16 a[208:209], v8 offset:14400\n"
    "v_mfma_f32_16x16x32_bf16 v[212:215], a[236:239], v[60:63], v[212:215]\n"
    "ds_read_b64_tr_b16 a[210:211], v8 offset:17984\n"
    "buffer_load_dwordx4 v[108:111], v1, s[56:59], s37 offen offset:3072 sc1\n"
    "v_mfma_f32_16x16x32_bf16 v[188:191], a[240:243], v[56:59], v[188:191]\n"
    "ds_read_b64_tr_b16 a[212:213], v8 offset:14432\n"
    "v_mfma_f32_16x16x32_bf16 v[216:219], a[240:243], v[60:63], v[216:219]\n"
    "ds_read_b64_tr_b16 a[214:215], v8 offset:18016\n"
    "s_waitcnt lgkmcnt(8)\n"
    "v_mfma_f32_16x16x32_bf16 v[192:195], a[244:247], v[56:59], v[192:195]\n"
    "ds_read_b64_tr_b16 a[216:217], v8 offset:14464\n"
    "v_mfma_f32_16x16x32_bf16 v[220:223], a[244:247], v[60:63], v[220:223]\n"
    "ds_read_b64_tr_b16 a[218:219], v8 offset:18048\n"
    "v_mfma_f32_16x16x32_bf16 v[196:199], a[248:251], v[56:59], v[196:199]\n"
    "ds_read_b64_tr_b16 a[220:221], v8 offset:14496\n"
    "v_mfma_f32_16x16x32_bf16 v[224:227], a[248:251], v[60:63], v[224:227]\n"
    "ds_read_b64_tr_b16 a[222:223], v8 offset:18080\n"
    "s_add_u32 m0, s10, 0x1c000\n"
    "s_add_u32 s31, s76, 0xc000\n"
    "buffer_load_dwordx4 v2, s[48:51], s31 offen lds\n"
    "buffer_load_dwordx4 v3, s[48:51], s31 offen offset:1024 lds\n"
    "v_mfma_f32_16x16x32_bf16 v[200:203], a[252:255], v[56:59], v[200:203]\n"
    "ds_read_b64_tr_b16 a[224:225], v8 offset:14528\n"
    "v_mfma_f32_16x16x32_bf16 v[228:231], a[252:255], v[60:63], v[228:231]\n"
    "ds_read_b64_tr_b16 a[226:227], v8 offset:18112\n"
    "buffer_load_dwordx4 v[128:131], v1, s[56:59], s34 offen sc1\n"
    "s_waitcnt lgkmcnt(6)\n"
    "s_waitcnt vmcnt(17)\n"
    "v_mfma_f32_16x16x32_bf16 v[176:179], a[200:203], v[64:67], v[176:179]\n"
    "ds_read_b64_tr_b16 a[228:229], v8 offset:21504\n"
    "v_mfma_f32_16x16x32_bf16 v[204:207], a[200:203], v[68:71], v[204:207]\n"
    "ds_read_b64_tr_b16 a[230:231], v8 offset:25088\n"
    "buffer_load_dwordx4 v[132:135], v1, s[56:59], s38 offen sc1\n"
    "v_mfma_f32_16x16x32_bf16 v[180:183], a[204:207], v[64:67], v[180:183]\n"
    "ds_read_b64_tr_b16 a[232:233], v8 offset:21536\n"
    "v_mfma_f32_16x16x32_bf16 v[208:211], a[204:207], v[68:71], v[208:211]\n"
    "ds_read_b64_tr_b16 a[234:235], v8 offset:25120\n"
    "s_barrier\n"
    "v_mfma_f32_16x16x32_bf16 v[184:187], a[208:211], v[64:67], v[184:187]\n"
    "ds_read_b64_tr_b16 a[236:237], v8 offset:21696\n"
    "v_mfma_f32_16x16x32_bf16 v[212:215], a[208:211], v[68:71], v[212:215]\n"
    "ds_read_b64_tr_b16 a[238:239], v8 offset:25280\n"
    "v_mfma_f32_16x16x32_bf16 v[188:191], a[212:215], v[64:67], v[188:191]\n"
    "ds_read_b64_tr_b16 a[240:241], v8 offset:28672\n"
    "v_mfma_f32_16x16x32_bf16 v[216:219], a[212:215], v[68:71], v[216:219]\n"
    "ds_read_b64_tr_b16 a[242:243], v8 offset:32256\n"
    "buffer_load_dwordx4 v[136:139], v1, s[56:59], s34 offen offset:1024 sc1\n"
    "s_waitcnt lgkmcnt(8)\n"
    "v_mfma_f32_16x16x32_bf16 v[192:195], a[216:219], v[64:67], v[192:195]\n"
    "ds_read_b64_tr_b16 a[244:245], v8 offset:28704\n"
    "v_mfma_f32_16x16x32_bf16 v[220:223], a[216:219], v[68:71], v[220:223]\n"
    "ds_read_b64_tr_b16 a[246:247], v8 offset:32288\n"
    "buffer_load_dwordx4 v[140:143], v1, s[56:59], s38 offen offset:1024 sc1\n"
    "v_mfma_f32_16x16x32_bf16 v[196:199], a[220:223], v[64:67], v[196:199]\n"
    "ds_read_b64_tr_b16 a[248:249], v8 offset:28864\n"
    "v_mfma_f32_16x16x32_bf16 v[224:227], a[220:223], v[68:71], v[224:227]\n"
    "ds_read_b64_tr_b16 a[250:251], v8 offset:32448\n"
    "s_add_u32 m0, s10, 0x1e000\n"
    "s_add_u32 s31, s76, 0x10000\n"
    "buffer_load_dwordx4 v2, s[48:51], s31 offen lds\n"
    "buffer_load_dwordx4 v3, s[48:51], s31 offen offset:1024 lds\n"
    "v_mfma_f32_16x16x32_bf16 v[200:203], a[224:227], v[64:67], v[200:203]\n"
    "ds_read_b64_tr_b16 a[252:253], v8 offset:35840\n"
    "v_mfma_f32_16x16x32_bf16 v[228:231], a[224:227], v[68:71], v[228:231]\n"
    "ds_read_b64_tr_b16 a[254:255], v8 offset:39424\n"
    "s_waitcnt lgkmcnt(8)\n"
    "s_waitcnt vmcnt(20)\n"
    "v_mfma_f32_16x16x32_bf16 v[176:179], a[228:231], v[72:75], v[176:179]\n"
    "ds_read_b64_tr_b16 a[200:201], v8 offset:35872\n"
    "v_mfma_f32_16x16x32_bf16 v[204:207], a[228:231], v[76:79], v[204:207]\n"
    "ds_read_b64_tr_b16 a[202:203], v8 offset:39456\n"
    "buffer_load_dwordx4 v[144:147], v1, s[56:59], s34 offen offset:2048 sc1\n"
    "v_mfma_f32_16x16x32_bf16 v[180:183], a[232:235], v[72:75], v[180:183]\n"
    "ds_read_b64_tr_b16 a[204:205], v8 offset:36032\n"
    "v_mfma_f32_16x16x32_bf16 v[208:211], a[232:235], v[76:79], v[208:211]\n"
    "ds_read_b64_tr_b16 a[206:207], v8 offset:39616\n"
    "buffer_load_dwordx4 v[148:151], v1, s[56:59], s38 offen offset:2048 sc1\n"
    "v_mfma_f32_16x16x32_bf16 v[200:203], a[236:239], v[72:75], v[200:203]\n"
    "ds_read_b64_tr_b16 a[208:209], v8 offset:43008\n"
    "v_mfma_f32_16x16x32_bf16 v[228:231], a[236:239], v[76:79], v[228:231]\n"
    "ds_read_b64_tr_b16 a[210:211], v8 offset:46592\n"
    "s_waitcnt lgkmcnt(8)\n"
    "s_waitcnt vmcnt(20)\n"
    "v_mfma_f32_16x16x32_bf16 v[176:179], a[240:243], v[80:83], v[176:179]\n"
    "ds_read_b64_tr_b16 a[212:213], v8 offset:43040\n"
    "v_mfma_f32_16x16x32_bf16 v[204:207], a[240:243], v[84:87], v[204:207]\n"
    "ds_read_b64_tr_b16 a[214:215], v8 offset:46624\n"
    "v_mfma_f32_16x16x32_bf16 v[180:183], a[244:247], v[80:83], v[180:183]\n"
    "ds_read_b64_tr_b16 a[216:217], v8 offset:43200\n"
    "v_mfma_f32_16x16x32_bf16 v[208:211], a[244:247], v[84:87], v[208:211]\n"
    "ds_read_b64_tr_b16 a[218:219], v8 offset:46784\n"
    "s_add_u32 m0, s10, 0x20000\n"
    "s_add_u32 s31, s76, 0x14000\n"
    "buffer_load_dwordx4 v2, s[48:51], s31 offen lds\n"
    "buffer_load_dwordx4 v3, s[48:51], s31 offen offset:1024 lds\n"
    "v_mfma_f32_16x16x32_bf16 v[200:203], a[248:251], v[80:83], v[200:203]\n"
    "ds_read_b64_tr_b16 a[220:221], v8 offset:50176\n"
    "v_mfma_f32_16x16x32_bf16 v[228:231], a[248:251], v[84:87], v[228:231]\n"
    "ds_read_b64_tr_b16 a[222:223], v8 offset:53760\n"
    "buffer_load_dwordx4 v[152:155], v1, s[56:59], s34 offen offset:3072 sc1\n"
    "s_waitcnt lgkmcnt(8)\n"
    "s_waitcnt vmcnt(21)\n"
    "v_mfma_f32_16x16x32_bf16 v[176:179], a[252:255], v[88:91], v[176:179]\n"
    "ds_read_b64_tr_b16 a[224:225], v8 offset:50208\n"
    "v_mfma_f32_16x16x32_bf16 v[204:207], a[252:255], v[92:95], v[204:207]\n"
    "ds_read_b64_tr_b16 a[226:227], v8 offset:53792\n"
    "buffer_load_dwordx4 v[156:159], v1, s[56:59], s38 offen offset:3072 sc1\n"
    "v_mfma_f32_16x16x32_bf16 v[180:183], a[200:203], v[88:91], v[180:183]\n"
    "ds_read_b64_tr_b16 a[228:229], v8 offset:50368\n"
    "v_mfma_f32_16x16x32_bf16 v[208:211], a[200:203], v[92:95], v[208:211]\n"
    "ds_read_b64_tr_b16 a[230:231], v8 offset:53952\n"
    "v_mfma_f32_16x16x32_bf16 v[200:203], a[204:207], v[88:91], v[200:203]\n"
    "ds_read_b64_tr_b16 a[232:233], v9\n"
    "v_mfma_f32_16x16x32_bf16 v[228:231], a[204:207], v[92:95], v[228:231]\n"
    "ds_read_b64_tr_b16 a[234:235], v9 offset:3584\n"
    "s_waitcnt lgkmcnt(8)\n"
    "s_waitcnt vmcnt(20)\n"
    "v_mfma_f32_16x16x32_bf16 v[176:179], a[208:211], v[96:99], v[176:179]\n"
    "ds_read_b64_tr_b16 a[236:237], v9 offset:32\n"
    "v_mfma_f32_16x16x32_bf16 v[204:207], a[208:211], v[100:103], v[204:207]\n"
    "ds_read_b64_tr_b16 a[238:239], v9 offset:3616\n"
    "buffer_load_dwordx4 v[160:163], v1, s[56:59], s35 offen sc1\n"
    "v_mfma_f32_16x16x32_bf16 v[180:183], a[212:215], v[96:99], v[180:183]\n"
    "ds_read_b64_tr_b16 a[240:241], v9 offset:192\n"
    "v_mfma_f32_16x16x32_bf16 v[208:211], a[212:215], v[100:103], v[208:211]\n"
    "ds_read_b64_tr_b16 a[242:243], v9 offset:3776\n"
    "buffer_load_dwordx4 v[164:167], v1, s[56:59], s39 offen sc1\n"
    "v_mfma_f32_16x16x32_bf16 v[200:203], a[216:219], v[96:99], v[200:203]\n"
    "ds_read_b64_tr_b16 a[244:245], v9 offset:7168\n"
    "v_mfma_f32_16x16x32_bf16 v[228:231], a[216:219], v[100:103], v[228:231]\n"
    "ds_read_b64_tr_b16 a[246:247], v9 offset:10752\n"
    "s_add_u32 m0, s10, 0x22000\n"
    "s_add_u32 s31, s77, 0\n"
    "buffer_load_dwordx4 v2, s[48:51], s31 offen lds\n"
    "buffer_load_dwordx4 v3, s[48:51], s31 offen offset:1024 lds\n"
    "s_waitcnt lgkmcnt(8)\n"
    "s_waitcnt vmcnt(18)\n"
    "v_mfma_f32_16x16x32_bf16 v[176:179], a[220:223], v[104:107], v[176:179]\n"
    "ds_read_b64_tr_b16 a[248:249], v9 offset:7200\n"
    "v_mfma_f32_16x16x32_bf16 v[204:207], a[220:223], v[108:111], v[204:207]\n"
    "ds_read_b64_tr_b16 a[250:251], v9 offset:10784\n"
    "v_mfma_f32_16x16x32_bf16 v[180:183], a[224:227], v[104:107], v[180:183]\n"
    "ds_read_b64_tr_b16 a[252:253], v9 offset:7360\n"
    "v_mfma_f32_16x16x32_bf16 v[208:211], a[224:227], v[108:111], v[208:211]\n"
    "ds_read_b64_tr_b16 a[254:255], v9 offset:10944\n"
    "buffer_load_dwordx4 v[168:171], v1, s[56:59], s35 offen offset:1024 sc1\n"
    "v_mfma_f32_16x16x32_bf16 v[200:203], a[228:231], v[104:107], v[200:203]\n"
    "ds_read_b64_tr_b16 a[200:201], v9 offset:14336\n"
    "v_mfma_f32_16x16x32_bf16 v[228:231], a[228:231], v[108:111], v[228:231]\n"
    "ds_read_b64_tr_b16 a[202:203], v9 offset:17920\n"
    "buffer_load_dwordx4 v[172:175], v1, s[56:59], s39 offen offset:1024 sc1\n"
    "s_waitcnt lgkmcnt(8)\n"
    "s_waitcnt vmcnt(16)\n"
    "v_mfma_f32_16x16x32_bf16 v[176:179], a[232:235], v[128:131], v[176:179]\n"
    "ds_read_b64_tr_b16 a[204:205], v9 offset:14368\n"
    "v_mfma_f32_16x16x32_bf16 v[204:207], a[232:235], v[132:135], v[204:207]\n"
    "ds_read_b64_tr_b16 a[206:207], v9 offset:17952\n"
    "v_mfma_f32_16x16x32_bf16 v[180:183], a[236:239], v[128:131], v[180:183]\n"
    "ds_read_b64_tr_b16 a[208:209], v9 offset:14528\n"
    "v_mfma_f32_16x16x32_bf16 v[208:211], a[236:239], v[132:135], v[208:211]\n"
    "ds_read_b64_tr_b16 a[210:211], v9 offset:18112\n"
    "v_mfma_f32_16x16x32_bf16 v[200:203], a[240:243], v[128:131], v[200:203]\n"
    "ds_read_b64_tr_b16 a[212:213], v9 offset:21504\n"
    "v_mfma_f32_16x16x32_bf16 v[228:231], a[240:243], v[132:135], v[228:231]\n"
    "ds_read_b64_tr_b16 a[214:215], v9 offset:25088\n"
    "s_add_u32 m0, s10, 0x24000\n"
    "s_add_u32 s31, s77, 0x4000\n"
    "buffer_load_dwordx4 v2, s[48:51], s31 offen lds\n"
    "buffer_load_dwordx4 v3, s[48:51], s31 offen offset:1024 lds\n"
    "s_waitcnt lgkmcnt(8)\n"
    "s_waitcnt vmcnt(16)\n"
    "v_mfma_f32_16x16x32_bf16 v[176:179], a[244:247], v[136:139], v[176:179]\n"
    "ds_read_b64_tr_b16 a[216:217], v9 offset:21536\n"
    "v_mfma_f32_16x16x32_bf16 v[204:207], a[244:247], v[140:143], v[204:207]\n"
    "ds_read_b64_tr_b16 a[218:219], v9 offset:25120\n"
    "buffer_load_dwordx4 v[48:51], v1, s[56:59], s35 offen offset:2048 sc1\n"
    "v_mfma_f32_16x16x32_bf16 v[180:183], a[248:251], v[136:139], v[180:183]\n"
    "ds_read_b64_tr_b16 a[220:221], v9 offset:21696\n"
    "v_mfma_f32_16x16x32_bf16 v[208:211], a[248:251], v[140:143], v[208:211]\n"
    "ds_read_b64_tr_b16 a[222:223], v9 offset:25280\n"
    "ds_read_b64_tr_b16 a[98:99], v4\n"
    "ds_read_b64_tr_b16 a[102:103], v5\n"
    "ds_read_b64_tr_b16 a[106:107], v6\n"
    "ds_read_b64_tr_b16 a[110:111], v7\n"
    "v_mfma_f32_16x16x32_bf16 v[200:203], a[252:255], v[136:139], v[200:203]\n"
    "ds_read_b64_tr_b16 a[224:225], v9 offset:28672\n"
    "v_mfma_f32_16x16x32_bf16 v[228:231], a[252:255], v[140:143], v[228:231]\n"
    "ds_read_b64_tr_b16 a[226:227], v9 offset:32256\n"
    "buffer_load_dwordx4 v[52:55], v1, s[56:59], s39 offen offset:2048 sc1\n"
    "s_waitcnt lgkmcnt(12)\n"
    "s_waitcnt vmcnt(14)\n"
    "v_mfma_f32_16x16x32_bf16 v[176:179], a[200:203], v[144:147], v[176:179]\n"
    "ds_read_b64_tr_b16 a[228:229], v9 offset:28704\n"
    "v_mfma_f32_16x16x32_bf16 v[204:207], a[200:203], v[148:151], v[204:207]\n"
    "ds_read_b64_tr_b16 a[230:231], v9 offset:32288\n"
    "buffer_load_dwordx4 v[56:59], v1, s[56:59], s35 offen offset:3072 sc1\n"
    "v_mfma_f32_16x16x32_bf16 v[180:183], a[204:207], v[144:147], v[180:183]\n"
    "ds_read_b64_tr_b16 a[232:233], v9 offset:28864\n"
    "v_mfma_f32_16x16x32_bf16 v[208:211], a[204:207], v[148:151], v[208:211]\n"
    "ds_read_b64_tr_b16 a[234:235], v9 offset:32448\n"
    "v_mfma_f32_16x16x32_bf16 v[200:203], a[208:211], v[144:147], v[200:203]\n"
    "ds_read_b64_tr_b16 a[236:237], v9 offset:35840\n"
    "v_mfma_f32_16x16x32_bf16 v[228:231], a[208:211], v[148:151], v[228:231]\n"
    "ds_read_b64_tr_b16 a[238:239], v9 offset:39424\n"
    "buffer_load_dwordx4 v[60:63], v1, s[56:59], s39 offen offset:3072 sc1\n"
    "s_waitcnt lgkmcnt(12)\n"
    "s_waitcnt vmcnt(12)\n"
    "v_mfma_f32_16x16x32_bf16 v[176:179], a[212:215], v[152:155], v[176:179]\n"
    "ds_read_b64_tr_b16 a[240:241], v9 offset:35872\n"
    "v_mfma_f32_16x16x32_bf16 v[204:207], a[212:215], v[156:159], v[204:207]\n"
    "ds_read_b64_tr_b16 a[242:243], v9 offset:39456\n"
    "s_waitcnt lgkmcnt(10)\n"
    "s_add_u32 m0, s10, 0x1c000\n"
    "s_add_u32 s31, s78, 0\n"
    "buffer_load_dwordx4 v2, s[48:51], s31 offen lds\n"
    "buffer_load_dwordx4 v3, s[48:51], s31 offen offset:1024 lds\n"
    "v_mfma_f32_16x16x32_bf16 v[180:183], a[216:219], v[152:155], v[180:183]\n"
    "ds_read_b64_tr_b16 a[244:245], v9 offset:36032\n"
    "v_mfma_f32_16x16x32_bf16 v[208:211], a[216:219], v[156:159], v[208:211]\n"
    "ds_read_b64_tr_b16 a[246:247], v9 offset:39616\n"
    "ds_read_b64_tr_b16 a[160:161], v4 offset:8192\n"
    "ds_read_b64_tr_b16 a[164:165], v5 offset:8192\n"
    "ds_read_b64_tr_b16 a[168:169], v6 offset:8192\n"
    "ds_read_b64_tr_b16 a[172:173], v7 offset:8192\n"
    "v_mfma_f32_16x16x32_bf16 v[200:203], a[220:223], v[152:155], v[200:203]\n"
    "ds_read_b64_tr_b16 a[248:249], v9 offset:43008\n"
    "v_mfma_f32_16x16x32_bf16 v[228:231], a[220:223], v[156:159], v[228:231]\n"
    "ds_read_b64_tr_b16 a[250:251], v9 offset:46592\n"
    "s_waitcnt lgkmcnt(12)\n"
    "s_waitcnt vmcnt(12)\n"
    "v_mfma_f32_16x16x32_bf16 v[176:179], a[224:227], v[160:163], v[176:179]\n"
    "ds_read_b64_tr_b16 a[252:253], v9 offset:43040\n"
    "v_mfma_f32_16x16x32_bf16 v[204:207], a[224:227], v[164:167], v[204:207]\n"
    "ds_read_b64_tr_b16 a[254:255], v9 offset:46624\n"
    "v_mfma_f32_16x16x32_bf16 v[180:183], a[228:231], v[160:163], v[180:183]\n"
    "ds_read_b64_tr_b16 a[200:201], v9 offset:43200\n"
    "v_mfma_f32_16x16x32_bf16 v[208:211], a[228:231], v[164:167], v[208:211]\n"
    "ds_read_b64_tr_b16 a[202:203], v9 offset:46784\n"
    "v_add_f32 v240, v240, v242\n"
    "v_add_f32 v241, v241, v243\n"
    "v_add_f32 v244, v244, v246\n"
    "v_add_f32 v245, v245, v247\n"
    "v_mov_b32 v242, v240\n"
    "v_mov_b32 v243, v241\n"
    "v_mov_b32 v246, v244\n"
    "v_mov_b32 v247, v245\n"
    "v_permlane16_swap_b32 v240, v242\n"
    "v_permlane16_swap_b32 v241, v243\n"
    "v_permlane16_swap_b32 v244, v246\n"
    "v_permlane16_swap_b32 v245, v247\n"
    "v_add_f32 v240, v240, v242\n"
    "v_add_f32 v241, v241, v243\n"
    "v_add_f32 v244, v244, v246\n"
    "v_add_f32 v245, v245, v247\n"
    "v_mov_b32 v242, v240\n"
    "v_mov_b32 v243, v241\n"
    "v_mov_b32 v246, v244\n"
    "v_mov_b32 v247, v245\n"
    "v_permlane32_swap_b32 v240, v242\n"
    "v_permlane32_swap_b32 v241, v243\n"
    "v_permlane32_swap_b32 v244, v246\n"
    "v_permlane32_swap_b32 v245, v247\n"
    "v_add_f32 v240, v240, v242\n"
    "v_add_f32 v241, v241, v243\n"
    "v_add_f32 v244, v244, v246\n"
    "v_add_f32 v245, v245, v247\n"
    "v_mul_f32 v232, 0x3b000000, v240\n"
    "v_mul_f32 v234, 0x3b000000, v244\n"
    "v_mul_f32 v242, 0x3b000000, v241\n"
    "v_mul_f32 v246, 0x3b000000, v245\n"
    "v_fma_f32 v242, -v232, v232, v242\n"
    "v_fma_f32 v246, -v234, v234, v246\n"
    "v_max_f32 v242, 0, v242\n"
    "v_max_f32 v246, 0, v246\n"
    "v_add_f32 v242, 0x3727c5ac, v242\n"
    "v_add_f32 v246, 0x3727c5ac, v246\n"
    "v_rsq_f32 v237, v242\n"
    "v_rsq_f32 v239, v246\n"
    "v_mul_f32 v233, v242, v237\n"
    "v_mul_f32 v235, v246, v239\n"
    "v_mul_f32 v236, 0xc0135761, v237\n"
    "v_mul_f32 v238, 0xc0135761, v239\n"
    "v_mul_f32 v237, v237, v237\n"
    "v_mul_f32 v239, v239, v239\n"
    "v_mul_f32 v237, 0x3d372713, v237\n"
    "v_mul_f32 v239, 0x3d372713, v239\n"
    "v_mul_f32 v237, v237, v236\n"
    "v_mul_f32 v239, v239, v238\n"
    "v_mfma_f32_16x16x32_bf16 v[200:203], a[232:235], v[160:163], v[200:203]\n"
    "ds_read_b64_tr_b16 a[204:205], v9 offset:50176\n"
    "v_mfma_f32_16x16x32_bf16 v[228:231], a[232:235], v[164:167], v[228:231]\n"
    "ds_read_b64_tr_b16 a[206:207], v9 offset:53760\n"
    "s_waitcnt lgkmcnt(12)\n"
    "s_waitcnt vmcnt(8)\n"
    "v_mfma_f32_16x16x32_bf16 v[176:179], a[236:239], v[168:171], v[176:179]\n"
    "ds_read_b64_tr_b16 a[208:209], v9 offset:50208\n"
    "v_mfma_f32_16x16x32_bf16 v[204:207], a[236:239], v[172:175], v[204:207]\n"
    "ds_read_b64_tr_b16 a[210:211], v9 offset:53792\n"
    "s_waitcnt lgkmcnt(10)\n"
    "s_add_u32 m0, s10, 0x1e000\n"
    "s_add_u32 s31, s78, 0x4000\n"
    "buffer_load_dwordx4 v2, s[48:51], s31 offen lds\n"
    "buffer_load_dwordx4 v3, s[48:51], s31 offen offset:1024 lds\n"
    "v_mfma_f32_16x16x32_bf16 v[180:183], a[240:243], v[168:171], v[180:183]\n"
    "ds_read_b64_tr_b16 a[212:213], v9 offset:50368\n"
    "v_mfma_f32_16x16x32_bf16 v[208:211], a[240:243], v[172:175], v[208:211]\n"
    "ds_read_b64_tr_b16 a[214:215], v9 offset:53952\n"
    "ds_read_b64_tr_b16 a[162:163], v4 offset:16384\n"
    "ds_read_b64_tr_b16 a[166:167], v5 offset:16384\n"
    "ds_read_b64_tr_b16 a[170:171], v6 offset:16384\n"
    "ds_read_b64_tr_b16 a[174:175], v7 offset:16384\n"
    "v_mfma_f32_16x16x32_bf16 v[200:203], a[244:247], v[168:171], v[200:203]\n"
    "ds_read_b64_tr_b16 a[216:217], v8 offset:21568\n"
    "v_mfma_f32_16x16x32_bf16 v[228:231], a[244:247], v[172:175], v[228:231]\n"
    "ds_read_b64_tr_b16 a[218:219], v8 offset:25152\n"
    "s_waitcnt lgkmcnt(12)\n"
    "s_waitcnt vmcnt(6)\n"
    "v_mfma_f32_16x16x32_bf16 v[176:179], a[248:251], v[48:51], v[176:179]\n"
    "ds_read_b64_tr_b16 a[220:221], v8 offset:21600\n"
    "v_mfma_f32_16x16x32_bf16 v[204:207], a[248:251], v[52:55], v[204:207]\n"
    "ds_read_b64_tr_b16 a[222:223], v8 offset:25184\n"
    "v_mfma_f32_16x16x32_bf16 v[180:183], a[252:255], v[48:51], v[180:183]\n"
    "ds_read_b64_tr_b16 a[224:225], v8 offset:28736\n"
    "v_mfma_f32_16x16x32_bf16 v[208:211], a[252:255], v[52:55], v[208:211]\n"
    "ds_read_b64_tr_b16 a[226:227], v8 offset:32320\n"
    "ds_read_b128 v[248:251], v23\n"
    "ds_read_b128 v[252:255], v23 offset:64\n"
    "ds_read_b128 v[36:39], v23 offset:384\n"
    "v_mfma_f32_16x16x32_bf16 v[200:203], a[200:203], v[48:51], v[200:203]\n"
    "ds_read_b64_tr_b16 a[228:229], v8 offset:28768\n"
    "v_mfma_f32_16x16x32_bf16 v[228:231], a[200:203], v[52:55], v[228:231]\n"
    "ds_read_b64_tr_b16 a[230:231], v8 offset:32352\n"
    "s_waitcnt lgkmcnt(15)\n"
    "s_waitcnt vmcnt(4)\n"
    "v_mfma_f32_16x16x32_bf16 v[176:179], a[204:207], v[56:59], v[176:179]\n"
    "ds_read_b64_tr_b16 a[232:233], v8 offset:35904\n"
    "v_mfma_f32_16x16x32_bf16 v[204:207], a[204:207], v[60:63], v[204:207]\n"
    "ds_read_b64_tr_b16 a[234:235], v8 offset:39488\n"
    "s_waitcnt lgkmcnt(13)\n"
    "s_add_u32 m0, s10, 0x20000\n"
    "s_add_u32 s31, s79, 0\n"
    "buffer_load_dwordx4 v2, s[48:51], s31 offen lds\n"
    "buffer_load_dwordx4 v3, s[48:51], s31 offen offset:1024 lds\n"
    "v_mfma_f32_16x16x32_bf16 v[180:183], a[208:211], v[56:59], v[180:183]\n"
    "ds_read_b64_tr_b16 a[236:237], v8 offset:35936\n"
    "v_mfma_f32_16x16x32_bf16 v[208:211], a[208:211], v[60:63], v[208:211]\n"
    "ds_read_b64_tr_b16 a[238:239], v8 offset:39520\n"
    "ds_read_b64_tr_b16 a[16:17], v4 offset:24576\n"
    "ds_read_b64_tr_b16 a[20:21], v5 offset:24576\n"
    "ds_read_b64_tr_b16 a[24:25], v6 offset:24576\n"
    "ds_read_b64_tr_b16 a[28:29], v7 offset:24576\n"
    "v_mfma_f32_16x16x32_bf16 v[200:203], a[212:215], v[56:59], v[200:203]\n"
    "ds_read_b64_tr_b16 a[240:241], v8 offset:43072\n"
    "v_mfma_f32_16x16x32_bf16 v[228:231], a[212:215], v[60:63], v[228:231]\n"
    "ds_read_b64_tr_b16 a[242:243], v8 offset:46656\n"
    "s_waitcnt lgkmcnt(12)\n"
    "v_pk_fma_f32 v[176:177], v[232:233], v[248:249], v[176:177] op_sel_hi:[0,1,1] neg_lo:[1,0,0] neg_hi:[1,0,0]\n"
    "v_pk_fma_f32 v[178:179], v[232:233], v[250:251], v[178:179] op_sel_hi:[0,1,1] neg_lo:[1,0,0] neg_hi:[1,0,0]\n"
    "v_pk_fma_f32 v[180:181], v[232:233], v[252:253], v[180:181] op_sel_hi:[0,1,1] neg_lo:[1,0,0] neg_hi:[1,0,0]\n"
    "v_pk_fma_f32 v[182:183], v[232:233], v[254:255], v[182:183] op_sel_hi:[0,1,1] neg_lo:[1,0,0] neg_hi:[1,0,0]\n"
    "v_pk_mul_f32 v[240:241], v[176:177], v[176:177]\n"
    "v_pk_mul_f32 v[242:243], v[178:179], v[178:179]\n"
    "v_pk_mul_f32 v[244:245], v[180:181], v[180:181]\n"
    "v_pk_mul_f32 v[246:247], v[182:183], v[182:183]\n"
    "v_pk_fma_f32 v[240:241], v[240:241], v[236:237], v[236:237] op_sel:[0,1,0] op_sel_hi:[1,1,0]\n"
    "v_pk_fma_f32 v[242:243], v[242:243], v[236:237], v[236:237] op_sel:[0,1,0] op_sel_hi:[1,1,0]\n"
    "v_pk_fma_f32 v[244:245], v[244:245], v[236:237], v[236:237] op_sel:[0,1,0] op_sel_hi:[1,1,0]\n"
    "v_pk_fma_f32 v[246:247], v[246:247], v[236:237], v[236:237] op_sel:[0,1,0] op_sel_hi:[1,1,0]\n"
    "v_pk_mul_f32 v[240:241], v[240:241], v[176:177]\n"
    "v_pk_mul_f32 v[242:243], v[242:243], v[178:179]\n"
    "v_pk_mul_f32 v[244:245], v[244:245], v[180:181]\n"
    "v_pk_mul_f32 v[246:247], v[246:247], v[182:183]\n"
    "v_exp_f32 v240, v240\n"
    "v_exp_f32 v241, v241\n"
    "v_exp_f32 v242, v242\n"
    "v_exp_f32 v243, v243\n"
    "v_exp_f32 v244, v244\n"
    "v_exp_f32 v245, v245\n"
    "v_exp_f32 v246, v246\n"
    "v_exp_f32 v247, v247\n"
    "s_waitcnt lgkmcnt(2)\n"
    "s_add_u32 m0, s10, 0x22000\n"
    "s_add_u32 s31, s79, 0x4000\n"
    "buffer_load_dwordx4 v2, s[48:51], s31 offen lds\n"
    "buffer_load_dwordx4 v3, s[48:51], s31 offen offset:1024 lds\n"
    "v_pk_fma_f32 v[240:241], v[240:241], v[232:233], v[232:233] op_sel:[0,1,1] op_sel_hi:[1,1,1]\n"
    "v_pk_fma_f32 v[242:243], v[242:243], v[232:233], v[232:233] op_sel:[0,1,1] op_sel_hi:[1,1,1]\n"
    "v_pk_fma_f32 v[244:245], v[244:245], v[232:233], v[232:233] op_sel:[0,1,1] op_sel_hi:[1,1,1]\n"
    "v_pk_fma_f32 v[246:247], v[246:247], v[232:233], v[232:233] op_sel:[0,1,1] op_sel_hi:[1,1,1]\n"
    "ds_read_b64_tr_b16 a[18:19], v4 offset:32768\n"
    "ds_read_b64_tr_b16 a[22:23], v5 offset:32768\n"
    "ds_read_b64_tr_b16 a[26:27], v6 offset:32768\n"
    "ds_read_b64_tr_b16 a[30:31], v7 offset:32768\n"
    "v_rcp_f32 v240, v240\n"
    "v_rcp_f32 v241, v241\n"
    "v_rcp_f32 v242, v242\n"
    "v_rcp_f32 v243, v243\n"
    "v_rcp_f32 v244, v244\n"
    "v_rcp_f32 v245, v245\n"
    "v_rcp_f32 v246, v246\n"
    "v_rcp_f32 v247, v247\n"
    "v_pk_mul_f32 v[176:177], v[176:177], v[240:241]\n"
    "v_pk_mul_f32 v[178:179], v[178:179], v[242:243]\n"
    "v_pk_mul_f32 v[180:181], v[180:181], v[244:245]\n"
    "v_pk_mul_f32 v[182:183], v[182:183], v[246:247]\n"
    "v_cvt_pk_bf16_f32 v176, v176, v177\n"
    "v_cvt_pk_bf16_f32 v177, v178, v179\n"
    "v_cvt_pk_bf16_f32 v178, v180, v181\n"
    "v_cvt_pk_bf16_f32 v179, v182, v183\n"
    "buffer_store_dwordx4 v[176:179], v1, s[56:59], s24 offen sc1\n"
    "v_pk_fma_f32 v[204:205], v[234:235], v[248:249], v[204:205] op_sel_hi:[0,1,1] neg_lo:[1,0,0] neg_hi:[1,0,0]\n"
    "v_pk_fma_f32 v[206:207], v[234:235], v[250:251], v[206:207] op_sel_hi:[0,1,1] neg_lo:[1,0,0] neg_hi:[1,0,0]\n"
    "v_pk_fma_f32 v[208:209], v[234:235], v[252:253], v[208:209] op_sel_hi:[0,1,1] neg_lo:[1,0,0] neg_hi:[1,0,0]\n"
    "v_pk_fma_f32 v[210:211], v[234:235], v[254:255], v[210:211] op_sel_hi:[0,1,1] neg_lo:[1,0,0] neg_hi:[1,0,0]\n"
    "v_pk_mul_f32 v[240:241], v[204:205], v[204:205]\n"
    "v_pk_mul_f32 v[242:243], v[206:207], v[206:207]\n"
    "v_pk_mul_f32 v[244:245], v[208:209], v[208:209]\n"
    "v_pk_mul_f32 v[246:247], v[210:211], v[210:211]\n"
    "v_pk_fma_f32 v[240:241], v[240:241], v[238:239], v[238:239] op_sel:[0,1,0] op_sel_hi:[1,1,0]\n"
    "v_pk_fma_f32 v[242:243], v[242:243], v[238:239], v[238:239] op_sel:[0,1,0] op_sel_hi:[1,1,0]\n"
    "v_pk_fma_f32 v[244:245], v[244:245], v[238:239], v[238:239] op_sel:[0,1,0] op_sel_hi:[1,1,0]\n"
    "v_pk_fma_f32 v[246:247], v[246:247], v[238:239], v[238:239] op_sel:[0,1,0] op_sel_hi:[1,1,0]\n"
    "v_pk_mul_f32 v[240:241], v[240:241], v[204:205]\n"
    "v_pk_mul_f32 v[242:243], v[242:243], v[206:207]\n"
    "v_pk_mul_f32 v[244:245], v[244:245], v[208:209]\n"
    "v_pk_mul_f32 v[246:247], v[246:247], v[210:211]\n"
    "s_waitcnt lgkmcnt(0)\n"
    "s_add_u32 m0, s10, 0x24000\n"
    "s_add_u32 s31, s85, 0x18000\n"
    "buffer_load_dwordx4 v2, s[48:51], s31 offen lds\n"
    "buffer_load_dwordx4 v3, s[48:51], s31 offen offset:1024 lds\n"
    "v_exp_f32 v240, v240\n"
    "v_exp_f32 v241, v241\n"
    "v_exp_f32 v242, v242\n"
    "v_exp_f32 v243, v243\n"
    "v_exp_f32 v244, v244\n"
    "v_exp_f32 v245, v245\n"
    "v_exp_f32 v246, v246\n"
    "v_exp_f32 v247, v247\n"
    "s_waitcnt vmcnt(9)\n"
    "ds_read_b64_tr_b16 a[32:33], v4\n"
    "ds_read_b64_tr_b16 a[36:37], v5\n"
    "ds_read_b64_tr_b16 a[40:41], v6\n"
    "ds_read_b64_tr_b16 a[44:45], v7\n"
    "v_pk_fma_f32 v[240:241], v[240:241], v[234:235], v[234:235] op_sel:[0,1,1] op_sel_hi:[1,1,1]\n"
    "v_pk_fma_f32 v[242:243], v[242:243], v[234:235], v[234:235] op_sel:[0,1,1] op_sel_hi:[1,1,1]\n"
    "v_pk_fma_f32 v[244:245], v[244:245], v[234:235], v[234:235] op_sel:[0,1,1] op_sel_hi:[1,1,1]\n"
    "v_pk_fma_f32 v[246:247], v[246:247], v[234:235], v[234:235] op_sel:[0,1,1] op_sel_hi:[1,1,1]\n"
    "v_rcp_f32 v240, v240\n"
    "v_rcp_f32 v241, v241\n"
    "v_rcp_f32 v242, v242\n"
    "v_rcp_f32 v243, v243\n"
    "v_rcp_f32 v244, v244\n"
    "v_rcp_f32 v245, v245\n"
    "v_rcp_f32 v246, v246\n"
    "v_rcp_f32 v247, v247\n"
    "v_pk_mul_f32 v[204:205], v[204:205], v[240:241]\n"
    "v_pk_mul_f32 v[206:207], v[206:207], v[242:243]\n"
    "v_pk_mul_f32 v[208:209], v[208:209], v[244:245]\n"
    "v_pk_mul_f32 v[210:211], v[210:211], v[246:247]\n"
    "v_cvt_pk_bf16_f32 v204, v204, v205\n"
    "v_cvt_pk_bf16_f32 v205, v206, v207\n"
    "v_cvt_pk_bf16_f32 v206, v208, v209\n"
    "v_cvt_pk_bf16_f32 v207, v210, v211\n"
    "ds_read_b128 v[248:251], v23 offset:128\n"
    "ds_read_b128 v[252:255], v23 offset:192\n"
    "buffer_store_dwordx4 v[204:207], v1, s[56:59], s24 offen offset:1024 sc1\n"
    "v_pk_fma_f32 v[200:201], v[232:233], v[36:37], v[200:201] op_sel_hi:[0,1,1] neg_lo:[1,0,0] neg_hi:[1,0,0]\n"
    "v_pk_fma_f32 v[202:203], v[232:233], v[38:39], v[202:203] op_sel_hi:[0,1,1] neg_lo:[1,0,0] neg_hi:[1,0,0]\n"
    "v_pk_fma_f32 v[228:229], v[234:235], v[36:37], v[228:229] op_sel_hi:[0,1,1] neg_lo:[1,0,0] neg_hi:[1,0,0]\n"
    "v_pk_fma_f32 v[230:231], v[234:235], v[38:39], v[230:231] op_sel_hi:[0,1,1] neg_lo:[1,0,0] neg_hi:[1,0,0]\n"
    "v_pk_mul_f32 v[240:241], v[200:201], v[200:201]\n"
    "v_pk_mul_f32 v[242:243], v[202:203], v[202:203]\n"
    "v_pk_mul_f32 v[244:245], v[228:229], v[228:229]\n"
    "v_pk_mul_f32 v[246:247], v[230:231], v[230:231]\n"
    "s_waitcnt lgkmcnt(2)\n"
    "s_add_u32 m0, s10, 0x1c000\n"
    "s_add_u32 s31, s85, 0x34000\n"
    "buffer_load_dwordx4 v2, s[48:51], s31 offen lds\n"
    "buffer_load_dwordx4 v3, s[48:51], s31 offen offset:1024 lds\n"
    "v_pk_fma_f32 v[240:241], v[240:241], v[236:237], v[236:237] op_sel:[0,1,0] op_sel_hi:[1,1,0]\n"
    "v_pk_fma_f32 v[242:243], v[242:243], v[236:237], v[236:237] op_sel:[0,1,0] op_sel_hi:[1,1,0]\n"
    "v_pk_fma_f32 v[244:245], v[244:245], v[238:239], v[238:239] op_sel:[0,1,0] op_sel_hi:[1,1,0]\n"
    "v_pk_fma_f32 v[246:247], v[246:247], v[238:239], v[238:239] op_sel:[0,1,0] op_sel_hi:[1,1,0]\n"
    "v_pk_mul_f32 v[240:241], v[240:241], v[200:201]\n"
    "v_pk_mul_f32 v[242:243], v[242:243], v[202:203]\n"
    "v_pk_mul_f32 v[244:245], v[244:245], v[228:229]\n"
    "v_pk_mul_f32 v[246:247], v[246:247], v[230:231]\n"
    "s_waitcnt vmcnt(10)\n"
    "ds_read_b64_tr_b16 a[34:35], v4 offset:8192\n"
    "ds_read_b64_tr_b16 a[38:39], v5 offset:8192\n"
    "ds_read_b64_tr_b16 a[42:43], v6 offset:8192\n"
    "ds_read_b64_tr_b16 a[46:47], v7 offset:8192\n"
    "v_exp_f32 v240, v240\n"
    "v_exp_f32 v241, v241\n"
    "v_exp_f32 v242, v242\n"
    "v_exp_f32 v243, v243\n"
    "v_exp_f32 v244, v244\n"
    "v_exp_f32 v245, v245\n"
    "v_exp_f32 v246, v246\n"
    "v_exp_f32 v247, v247\n"
    "v_pk_fma_f32 v[240:241], v[240:241], v[232:233], v[232:233] op_sel:[0,1,1] op_sel_hi:[1,1,1]\n"
    "v_pk_fma_f32 v[242:243], v[242:243], v[232:233], v[232:233] op_sel:[0,1,1] op_sel_hi:[1,1,1]\n"
    "v_pk_fma_f32 v[244:245], v[244:245], v[234:235], v[234:235] op_sel:[0,1,1] op_sel_hi:[1,1,1]\n"
    "v_pk_fma_f32 v[246:247], v[246:247], v[234:235], v[234:235] op_sel:[0,1,1] op_sel_hi:[1,1,1]\n"
    "v_rcp_f32 v240, v240\n"
    "v_rcp_f32 v241, v241\n"
    "v_rcp_f32 v242, v242\n"
    "v_rcp_f32 v243, v243\n"
    "v_rcp_f32 v244, v244\n"
    "v_rcp_f32 v245, v245\n"
    "v_rcp_f32 v246, v246\n"
    "v_rcp_f32 v247, v247\n"
    "v_pk_mul_f32 v[200:201], v[200:201], v[240:241]\n"
    "v_pk_mul_f32 v[202:203], v[202:203], v[242:243]\n"
    "v_pk_mul_f32 v[228:229], v[228:229], v[244:245]\n"
    "v_pk_mul_f32 v[230:231], v[230:231], v[246:247]\n"
    "v_cvt_pk_bf16_f32 v200, v200, v201\n"
    "v_cvt_pk_bf16_f32 v201, v202, v203\n"
    "v_cvt_pk_bf16_f32 v202, v228, v229\n"
    "v_cvt_pk_bf16_f32 v203, v230, v231\n"
    "buffer_store_dwordx4 v[200:203], v1, s[56:59], s91 offen offset:2048 sc1\n"
    "v_mfma_f32_16x16x32_bf16 v[184:187], a[216:219], v[72:75], v[184:187]\n"
    "ds_read_b64_tr_b16 a[244:245], v8 offset:43104\n"
    "v_mfma_f32_16x16x32_bf16 v[212:215], a[216:219], v[76:79], v[212:215]\n"
    "ds_read_b64_tr_b16 a[246:247], v8 offset:46688\n"
    "s_waitcnt lgkmcnt(2)\n"
    "s_add_u32 m0, s10, 0x1e000\n"
    "s_add_u32 s31, s77, 0x8000\n"
    "buffer_load_dwordx4 v2, s[48:51], s31 offen lds\n"
    "buffer_load_dwordx4 v3, s[48:51], s31 offen offset:1024 lds\n"
    "v_mfma_f32_16x16x32_bf16 v[188:191], a[220:223], v[72:75], v[188:191]\n"
    "ds_read_b64_tr_b16 a[248:249], v8 offset:50240\n"
    "v_mfma_f32_16x16x32_bf16 v[216:219], a[220:223], v[76:79], v[216:219]\n"
    "ds_read_b64_tr_b16 a[250:251], v8 offset:53824\n"
    "s_waitcnt vmcnt(11)\n"
    "ds_read_b64_tr_b16 a[48:49], v4 offset:16384\n"
    "ds_read_b64_tr_b16 a[52:53], v5 offset:16384\n"
    "ds_read_b64_tr_b16 a[56:57], v6 offset:16384\n"
    "ds_read_b64_tr_b16 a[60:61], v7 offset:16384\n"
    "v_mfma_f32_16x16x32_bf16 v[184:187], a[224:227], v[80:83], v[184:187]\n"
    "ds_read_b64_tr_b16 a[252:253], v8 offset:50272\n"
    "v_mfma_f32_16x16x32_bf16 v[212:215], a[224:227], v[84:87], v[212:215]\n"
    "ds_read_b64_tr_b16 a[254:255], v8 offset:53856\n"
    "v_mfma_f32_16x16x32_bf16 v[188:191], a[228:231], v[80:83], v[188:191]\n"
    "ds_read_b64_tr_b16 a[200:201], v9 offset:64\n"
    "v_mfma_f32_16x16x32_bf16 v[216:219], a[228:231], v[84:87], v[216:219]\n"
    "ds_read_b64_tr_b16 a[202:203], v9 offset:3648\n"
    "v_mfma_f32_16x16x32_bf16 v[184:187], a[232:235], v[88:91], v[184:187]\n"
    "ds_read_b64_tr_b16 a[204:205], v9 offset:96\n"
    "v_mfma_f32_16x16x32_bf16 v[212:215], a[232:235], v[92:95], v[212:215]\n"
    "ds_read_b64_tr_b16 a[206:207], v9 offset:3680\n"
    "v_mfma_f32_16x16x32_bf16 v[188:191], a[236:239], v[88:91], v[188:191]\n"
    "ds_read_b64_tr_b16 a[208:209], v9 offset:7232\n"
    "v_mfma_f32_16x16x32_bf16 v[216:219], a[236:239], v[92:95], v[216:219]\n"
    "ds_read_b64_tr_b16 a[210:211], v9 offset:10816\n"
    "s_waitcnt lgkmcnt(14)\n"
    "v_mfma_f32_16x16x32_bf16 v[184:187], a[240:243], v[96:99], v[184:187]\n"
    "ds_read_b64_tr_b16 a[212:213], v9 offset:7264\n"
    "v_mfma_f32_16x16x32_bf16 v[212:215], a[240:243], v[100:103], v[212:215]\n"
    "ds_read_b64_tr_b16 a[214:215], v9 offset:10848\n"
    "s_waitcnt lgkmcnt(10)\n"
    "s_add_u32 m0, s10, 0x20000\n"
    "s_add_u32 s31, s77, 0xc000\n"
    "buffer_load_dwordx4 v2, s[48:51], s31 offen lds\n"
    "buffer_load_dwordx4 v3, s[48:51], s31 offen offset:1024 lds\n"
    "v_mfma_f32_16x16x32_bf16 v[188:191], a[244:247], v[96:99], v[188:191]\n"
    "ds_read_b64_tr_b16 a[216:217], v9 offset:14400\n"
    "v_mfma_f32_16x16x32_bf16 v[216:219], a[244:247], v[100:103], v[216:219]\n"
    "ds_read_b64_tr_b16 a[218:219], v9 offset:17984\n"
    "s_waitcnt lgkmcnt(10)\n"
    "v_mfma_f32_16x16x32_bf16 v[184:187], a[248:251], v[104:107], v[184:187]\n"
    "ds_read_b64_tr_b16 a[220:221], v9 offset:14432\n"
    "v_mfma_f32_16x16x32_bf16 v[212:215], a[248:251], v[108:111], v[212:215]\n"
    "ds_read_b64_tr_b16 a[222:223], v9 offset:18016\n"
    "s_waitcnt vmcnt(11)\n"
    "ds_read_b64_tr_b16 a[50:51], v4 offset:24576\n"
    "ds_read_b64_tr_b16 a[54:55], v5 offset:24576\n"
    "ds_read_b64_tr_b16 a[58:59], v6 offset:24576\n"
    "ds_read_b64_tr_b16 a[62:63], v7 offset:24576\n"
    "v_mfma_f32_16x16x32_bf16 v[188:191], a[252:255], v[104:107], v[188:191]\n"
    "ds_read_b64_tr_b16 a[224:225], v9 offset:21568\n"
    "v_mfma_f32_16x16x32_bf16 v[216:219], a[252:255], v[108:111], v[216:219]\n"
    "ds_read_b64_tr_b16 a[226:227], v9 offset:25152\n"
    "s_waitcnt lgkmcnt(14)\n"
    "v_mfma_f32_16x16x32_bf16 v[184:187], a[200:203], v[128:131], v[184:187]\n"
    "ds_read_b64_tr_b16 a[228:229], v9 offset:21600\n"
    "v_mfma_f32_16x16x32_bf16 v[212:215], a[200:203], v[132:135], v[212:215]\n"
    "ds_read_b64_tr_b16 a[230:231], v9 offset:25184\n"
    "v_mfma_f32_16x16x32_bf16 v[188:191], a[204:207], v[128:131], v[188:191]\n"
    "ds_read_b64_tr_b16 a[232:233], v9 offset:28736\n"
    "v_mfma_f32_16x16x32_bf16 v[216:219], a[204:207], v[132:135], v[216:219]\n"
    "ds_read_b64_tr_b16 a[234:235], v9 offset:32320\n"
    "s_waitcnt lgkmcnt(14)\n"
    "v_mfma_f32_16x16x32_bf16 v[184:187], a[208:211], v[136:139], v[184:187]\n"
    "ds_read_b64_tr_b16 a[236:237], v9 offset:28768\n"
    "v_mfma_f32_16x16x32_bf16 v[212:215], a[208:211], v[140:143], v[212:215]\n"
    "ds_read_b64_tr_b16 a[238:239], v9 offset:32352\n"
    "v_mfma_f32_16x16x32_bf16 v[188:191], a[212:215], v[136:139], v[188:191]\n"
    "ds_read_b64_tr_b16 a[240:241], v9 offset:35904\n"
    "v_mfma_f32_16x16x32_bf16 v[216:219], a[212:215], v[140:143], v[216:219]\n"
    "ds_read_b64_tr_b16 a[242:243], v9 offset:39488\n"
    "s_waitcnt lgkmcnt(10)\n"
    "s_add_u32 m0, s10, 0x22000\n"
    "s_add_u32 s31, s78, 0x8000\n"
    "buffer_load_dwordx4 v2, s[48:51], s31 offen lds\n"
    "buffer_load_dwordx4 v3, s[48:51], s31 offen offset:1024 lds\n"
    "v_mfma_f32_16x16x32_bf16 v[184:187], a[216:219], v[144:147], v[184:187]\n"
    "ds_read_b64_tr_b16 a[244:245], v9 offset:35936\n"
    "v_mfma_f32_16x16x32_bf16 v[212:215], a[216:219], v[148:151], v[212:215]\n"
    "ds_read_b64_tr_b16 a[246:247], v9 offset:39520\n"
    "s_waitcnt vmcnt(10)\n"
    "ds_read_b64_tr_b16 a[80:81], v4 offset:32768\n"
    "ds_read_b64_tr_b16 a[84:85], v5 offset:32768\n"
    "ds_read_b64_tr_b16 a[88:89], v6 offset:32768\n"
    "ds_read_b64_tr_b16 a[92:93], v7 offset:32768\n"
    "v_mfma_f32_16x16x32_bf16 v[188:191], a[220:223], v[144:147], v[188:191]\n"
    "ds_read_b64_tr_b16 a[248:249], v9 offset:43072\n"
    "v_mfma_f32_16x16x32_bf16 v[216:219], a[220:223], v[148:151], v[216:219]\n"
    "ds_read_b64_tr_b16 a[250:251], v9 offset:46656\n"
    "s_waitcnt lgkmcnt(14)\n"
    "v_mfma_f32_16x16x32_bf16 v[184:187], a[224:227], v[152:155], v[184:187]\n"
    "ds_read_b64_tr_b16 a[252:253], v9 offset:43104\n"
    "v_mfma_f32_16x16x32_bf16 v[212:215], a[224:227], v[156:159], v[212:215]\n"
    "ds_read_b64_tr_b16 a[254:255], v9 offset:46688\n"
    "v_mfma_f32_16x16x32_bf16 v[188:191], a[228:231], v[152:155], v[188:191]\n"
    "ds_read_b64_tr_b16 a[200:201], v9 offset:50240\n"
    "v_mfma_f32_16x16x32_bf16 v[216:219], a[228:231], v[156:159], v[216:219]\n"
    "ds_read_b64_tr_b16 a[202:203], v9 offset:53824\n"
    "s_waitcnt vmcnt(6)\n"
    "buffer_store_dwordx2 v[24:25], v17, s[56:59], s25 offen sc1\n"
    "s_waitcnt lgkmcnt(14)\n"
    "v_mfma_f32_16x16x32_bf16 v[184:187], a[232:235], v[160:163], v[184:187]\n"
    "ds_read_b64_tr_b16 a[204:205], v9 offset:50272\n"
    "v_mfma_f32_16x16x32_bf16 v[212:215], a[232:235], v[164:167], v[212:215]\n"
    "ds_read_b64_tr_b16 a[206:207], v9 offset:53856\n"
    "v_mfma_f32_16x16x32_bf16 v[188:191], a[236:239], v[160:163], v[188:191]\n"
    "ds_read_b64_tr_b16 a[208:209], v8 offset:21632\n"
    "v_mfma_f32_16x16x32_bf16 v[216:219], a[236:239], v[164:167], v[216:219]\n"
    "ds_read_b64_tr_b16 a[210:211], v8 offset:25216\n"
    "s_waitcnt lgkmcnt(10)\n"
    "s_add_u32 m0, s10, 0x24000\n"
    "s_add_u32 s31, s78, 0xc000\n"
    "buffer_load_dwordx4 v2, s[48:51], s31 offen lds\n"
    "buffer_load_dwordx4 v3, s[48:51], s31 offen offset:1024 lds\n"
    "v_mfma_f32_16x16x32_bf16 v[184:187], a[240:243], v[168:171], v[184:187]\n"
    "ds_read_b64_tr_b16 a[212:213], v8 offset:21664\n"
    "v_mfma_f32_16x16x32_bf16 v[212:215], a[240:243], v[172:175], v[212:215]\n"
    "ds_read_b64_tr_b16 a[214:215], v8 offset:25248\n"
    "ds_read_b64_tr_b16 a[82:83], v4\n"
    "ds_read_b64_tr_b16 a[86:87], v5\n"
    "ds_read_b64_tr_b16 a[90:91], v6\n"
    "ds_read_b64_tr_b16 a[94:95], v7\n"
    "v_mfma_f32_16x16x32_bf16 v[188:191], a[244:247], v[168:171], v[188:191]\n"
    "ds_read_b64_tr_b16 a[216:217], v8 offset:28800\n"
    "v_mfma_f32_16x16x32_bf16 v[216:219], a[244:247], v[172:175], v[216:219]\n"
    "ds_read_b64_tr_b16 a[218:219], v8 offset:32384\n"
    "s_waitcnt lgkmcnt(14)\n"
    "v_mfma_f32_16x16x32_bf16 v[184:187], a[248:251], v[48:51], v[184:187]\n"
    "ds_read_b64_tr_b16 a[220:221], v8 offset:28832\n"
    "v_mfma_f32_16x16x32_bf16 v[212:215], a[248:251], v[52:55], v[212:215]\n"
    "ds_read_b64_tr_b16 a[222:223], v8 offset:32416\n"
    "v_mfma_f32_16x16x32_bf16 v[188:191], a[252:255], v[48:51], v[188:191]\n"
    "ds_read_b64_tr_b16 a[224:225], v8 offset:35968\n"
    "v_mfma_f32_16x16x32_bf16 v[216:219], a[252:255], v[52:55], v[216:219]\n"
    "ds_read_b64_tr_b16 a[226:227], v8 offset:39552\n"
    "s_waitcnt lgkmcnt(14)\n"
    "v_mfma_f32_16x16x32_bf16 v[184:187], a[200:203], v[56:59], v[184:187]\n"
    "ds_read_b64_tr_b16 a[228:229], v8 offset:36000\n"
    "v_mfma_f32_16x16x32_bf16 v[212:215], a[200:203], v[60:63], v[212:215]\n"
    "ds_read_b64_tr_b16 a[230:231], v8 offset:39584\n"
    "v_mfma_f32_16x16x32_bf16 v[188:191], a[204:207], v[56:59], v[188:191]\n"
    "ds_read_b64_tr_b16 a[232:233], v8 offset:43136\n"
    "v_mfma_f32_16x16x32_bf16 v[216:219], a[204:207], v[60:63], v[216:219]\n"
    "ds_read_b64_tr_b16 a[234:235], v8 offset:46720\n"
    "s_nop 0\n"
    "v_pk_fma_f32 v[184:185], v[232:233], v[248:249], v[184:185] op_sel_hi:[0,1,1] neg_lo:[1,0,0] neg_hi:[1,0,0]\n"
    "v_pk_fma_f32 v[186:187], v[232:233], v[250:251], v[186:187] op_sel_hi:[0,1,1] neg_lo:[1,0,0] neg_hi:[1,0,0]\n"
    "s_nop 1\n"
    "v_pk_fma_f32 v[188:189], v[232:233], v[252:253], v[188:189] op_sel_hi:[0,1,1] neg_lo:[1,0,0] neg_hi:[1,0,0]\n"
    "v_pk_fma_f32 v[190:191], v[232:233], v[254:255], v[190:191] op_sel_hi:[0,1,1] neg_lo:[1,0,0] neg_hi:[1,0,0]\n"
    "s_waitcnt lgkmcnt(10)\n"
    "s_add_u32 m0, s10, 0x1c000\n"
    "s_add_u32 s31, s79, 0x8000\n"
    "buffer_load_dwordx4 v2, s[48:51], s31 offen lds\n"
    "buffer_load_dwordx4 v3, s[48:51], s31 offen offset:1024 lds\n"
    "v_pk_mul_f32 v[240:241], v[184:185], v[184:185]\n"
    "v_pk_mul_f32 v[242:243], v[186:187], v[186:187]\n"
    "v_pk_mul_f32 v[244:245], v[188:189], v[188:189]\n"
    "v_pk_mul_f32 v[246:247], v[190:191], v[190:191]\n"
    "s_waitcnt vmcnt(9)\n"
    "ds_read_b64_tr_b16 a[112:113], v4 offset:8192\n"
    "ds_read_b64_tr_b16 a[116:117], v5 offset:8192\n"
    "ds_read_b64_tr_b16 a[120:121], v6 offset:8192\n"
    "ds_read_b64_tr_b16 a[124:125], v7 offset:8192\n"
    "v_pk_fma_f32 v[240:241], v[240:241], v[236:237], v[236:237] op_sel:[0,1,0] op_sel_hi:[1,1,0]\n"
    "v_pk_fma_f32 v[242:243], v[242:243], v[236:237], v[236:237] op_sel:[0,1,0] op_sel_hi:[1,1,0]\n"
    "v_pk_fma_f32 v[244:245], v[244:245], v[236:237], v[236:237] op_sel:[0,1,0] op_sel_hi:[1,1,0]\n"
    "v_pk_fma_f32 v[246:247], v[246:247], v[236:237], v[236:237] op_sel:[0,1,0] op_sel_hi:[1,1,0]\n"
    "v_pk_mul_f32 v[240:241], v[240:241], v[184:185]\n"
    "v_pk_mul_f32 v[242:243], v[242:243], v[186:187]\n"
    "v_pk_mul_f32 v[244:245], v[244:245], v[188:189]\n"
    "v_pk_mul_f32 v[246:247], v[246:247], v[190:191]\n"
    "v_exp_f32 v240, v240\n"
    "v_exp_f32 v241, v241\n"
    "v_exp_f32 v242, v242\n"
    "v_exp_f32 v243, v243\n"
    "v_exp_f32 v244, v244\n"
    "v_exp_f32 v245, v245\n"
    "v_exp_f32 v246, v246\n"
    "v_exp_f32 v247, v247\n"
    "v_pk_fma_f32 v[240:241], v[240:241], v[232:233], v[232:233] op_sel:[0,1,1] op_sel_hi:[1,1,1]\n"
    "v_pk_fma_f32 v[242:243], v[242:243], v[232:233], v[232:233] op_sel:[0,1,1] op_sel_hi:[1,1,1]\n"
    "v_pk_fma_f32 v[244:245], v[244:245], v[232:233], v[232:233] op_sel:[0,1,1] op_sel_hi:[1,1,1]\n"
    "v_pk_fma_f32 v[246:247], v[246:247], v[232:233], v[232:233] op_sel:[0,1,1] op_sel_hi:[1,1,1]\n"
    "v_rcp_f32 v240, v240\n"
    "v_rcp_f32 v241, v241\n"
    "v_rcp_f32 v242, v242\n"
    "v_rcp_f32 v243, v243\n"
    "v_rcp_f32 v244, v244\n"
    "v_rcp_f32 v245, v245\n"
    "v_rcp_f32 v246, v246\n"
    "v_rcp_f32 v247, v247\n"
    "v_pk_mul_f32 v[184:185], v[184:185], v[240:241]\n"
    "v_pk_mul_f32 v[186:187], v[186:187], v[242:243]\n"
    "v_pk_mul_f32 v[188:189], v[188:189], v[244:245]\n"
    "v_pk_mul_f32 v[190:191], v[190:191], v[246:247]\n"
    "s_waitcnt lgkmcnt(0)\n"
    "s_add_u32 m0, s10, 0x1e000\n"
    "s_add_u32 s31, s79, 0xc000\n"
    "buffer_load_dwordx4 v2, s[48:51], s31 offen lds\n"
    "buffer_load_dwordx4 v3, s[48:51], s31 offen offset:1024 lds\n"
    "v_cvt_pk_bf16_f32 v184, v184, v185\n"
    "v_cvt_pk_bf16_f32 v185, v186, v187\n"
    "v_cvt_pk_bf16_f32 v186, v188, v189\n"
    "v_cvt_pk_bf16_f32 v187, v190, v191\n"
    "buffer_store_dwordx4 v[184:187], v1, s[56:59], s24 offen offset:2048 sc1\n"
    "v_pk_fma_f32 v[212:213], v[234:235], v[248:249], v[212:213] op_sel_hi:[0,1,1] neg_lo:[1,0,0] neg_hi:[1,0,0]\n"
    "v_pk_fma_f32 v[214:215], v[234:235], v[250:251], v[214:215] op_sel_hi:[0,1,1] neg_lo:[1,0,0] neg_hi:[1,0,0]\n"
    "v_pk_fma_f32 v[216:217], v[234:235], v[252:253], v[216:217] op_sel_hi:[0,1,1] neg_lo:[1,0,0] neg_hi:[1,0,0]\n"
    "v_pk_fma_f32 v[218:219], v[234:235], v[254:255], v[218:219] op_sel_hi:[0,1,1] neg_lo:[1,0,0] neg_hi:[1,0,0]\n"
    "s_waitcnt vmcnt(10)\n"
    "ds_read_b64_tr_b16 a[114:115], v4 offset:16384\n"
    "ds_read_b64_tr_b16 a[118:119], v5 offset:16384\n"
    "ds_read_b64_tr_b16 a[122:123], v6 offset:16384\n"
    "ds_read_b64_tr_b16 a[126:127], v7 offset:16384\n"
    "v_pk_mul_f32 v[240:241], v[212:213], v[212:213]\n"
    "v_pk_mul_f32 v[242:243], v[214:215], v[214:215]\n"
    "v_pk_mul_f32 v[244:245], v[216:217], v[216:217]\n"
    "v_pk_mul_f32 v[246:247], v[218:219], v[218:219]\n"
    "v_pk_fma_f32 v[240:241], v[240:241], v[238:239], v[238:239] op_sel:[0,1,0] op_sel_hi:[1,1,0]\n"
    "v_pk_fma_f32 v[242:243], v[242:243], v[238:239], v[238:239] op_sel:[0,1,0] op_sel_hi:[1,1,0]\n"
    "v_pk_fma_f32 v[244:245], v[244:245], v[238:239], v[238:239] op_sel:[0,1,0] op_sel_hi:[1,1,0]\n"
    "v_pk_fma_f32 v[246:247], v[246:247], v[238:239], v[238:239] op_sel:[0,1,0] op_sel_hi:[1,1,0]\n"
    "v_pk_mul_f32 v[240:241], v[240:241], v[212:213]\n"
    "v_pk_mul_f32 v[242:243], v[242:243], v[214:215]\n"
    "v_pk_mul_f32 v[244:245], v[244:245], v[216:217]\n"
    "v_pk_mul_f32 v[246:247], v[246:247], v[218:219]\n"
    "v_exp_f32 v240, v240\n"
    "v_exp_f32 v241, v241\n"
    "v_exp_f32 v242, v242\n"
    "v_exp_f32 v243, v243\n"
    "v_exp_f32 v244, v244\n"
    "v_exp_f32 v245, v245\n"
    "v_exp_f32 v246, v246\n"
    "v_exp_f32 v247, v247\n"
    "v_pk_fma_f32 v[240:241], v[240:241], v[234:235], v[234:235] op_sel:[0,1,1] op_sel_hi:[1,1,1]\n"
    "v_pk_fma_f32 v[242:243], v[242:243], v[234:235], v[234:235] op_sel:[0,1,1] op_sel_hi:[1,1,1]\n"
    "v_pk_fma_f32 v[244:245], v[244:245], v[234:235], v[234:235] op_sel:[0,1,1] op_sel_hi:[1,1,1]\n"
    "v_pk_fma_f32 v[246:247], v[246:247], v[234:235], v[234:235] op_sel:[0,1,1] op_sel_hi:[1,1,1]\n"
    "v_rcp_f32 v240, v240\n"
    "v_rcp_f32 v241, v241\n"
    "v_rcp_f32 v242, v242\n"
    "v_rcp_f32 v243, v243\n"
    "v_rcp_f32 v244, v244\n"
    "v_rcp_f32 v245, v245\n"
    "v_rcp_f32 v246, v246\n"
    "v_rcp_f32 v247, v247\n"
    "s_waitcnt lgkmcnt(0)\n"
    "s_add_u32 m0, s10, 0x20000\n"
    "s_add_u32 s31, s77, 0x10000\n"
    "buffer_load_dwordx4 v2, s[48:51], s31 offen lds\n"
    "buffer_load_dwordx4 v3, s[48:51], s31 offen offset:1024 lds\n"
    "v_pk_mul_f32 v[212:213], v[212:213], v[240:241]\n"
    "v_pk_mul_f32 v[214:215], v[214:215], v[242:243]\n"
    "v_pk_mul_f32 v[216:217], v[216:217], v[244:245]\n"
    "v_pk_mul_f32 v[218:219], v[218:219], v[246:247]\n"
    "s_waitcnt vmcnt(10)\n"
    "ds_read_b64_tr_b16 a[128:129], v4 offset:24576\n"
    "ds_read_b64_tr_b16 a[132:133], v5 offset:24576\n"
    "ds_read_b64_tr_b16 a[136:137], v6 offset:24576\n"
    "ds_read_b64_tr_b16 a[140:141], v7 offset:24576\n"
    "v_cvt_pk_bf16_f32 v212, v212, v213\n"
    "v_cvt_pk_bf16_f32 v213, v214, v215\n"
    "v_cvt_pk_bf16_f32 v214, v216, v217\n"
    "v_cvt_pk_bf16_f32 v215, v218, v219\n"
    "ds_read_b128 v[248:251], v23 offset:256\n"
    "ds_read_b128 v[252:255], v23 offset:320\n"
    "buffer_store_dwordx4 v[212:215], v1, s[56:59], s24 offen offset:3072 sc1\n"
    "v_mfma_f32_16x16x32_bf16 v[192:195], a[208:211], v[72:75], v[192:195]\n"
    "ds_read_b64_tr_b16 a[236:237], v8 offset:43168\n"
    "v_mfma_f32_16x16x32_bf16 v[220:223], a[208:211], v[76:79], v[220:223]\n"
    "ds_read_b64_tr_b16 a[238:239], v8 offset:46752\n"
    "v_mfma_f32_16x16x32_bf16 v[196:199], a[212:215], v[72:75], v[196:199]\n"
    "ds_read_b64_tr_b16 a[240:241], v8 offset:50304\n"
    "v_mfma_f32_16x16x32_bf16 v[224:227], a[212:215], v[76:79], v[224:227]\n"
    "ds_read_b64_tr_b16 a[242:243], v8 offset:53888\n"
    "v_mfma_f32_16x16x32_bf16 v[192:195], a[216:219], v[80:83], v[192:195]\n"
    "ds_read_b64_tr_b16 a[244:245], v8 offset:50336\n"
    "v_mfma_f32_16x16x32_bf16 v[220:223], a[216:219], v[84:87], v[220:223]\n"
    "ds_read_b64_tr_b16 a[246:247], v8 offset:53920\n"
    "v_mfma_f32_16x16x32_bf16 v[196:199], a[220:223], v[80:83], v[196:199]\n"
    "ds_read_b64_tr_b16 a[248:249], v9 offset:128\n"
    "v_mfma_f32_16x16x32_bf16 v[224:227], a[220:223], v[84:87], v[224:227]\n"
    "ds_read_b64_tr_b16 a[250:251], v9 offset:3712\n"
    "v_mfma_f32_16x16x32_bf16 v[192:195], a[224:227], v[88:91], v[192:195]\n"
    "ds_read_b64_tr_b16 a[252:253], v9 offset:160\n"
    "v_mfma_f32_16x16x32_bf16 v[220:223], a[224:227], v[92:95], v[220:223]\n"
    "ds_read_b64_tr_b16 a[254:255], v9 offset:3744\n"
    "s_waitcnt lgkmcnt(12)\n"
    "s_add_u32 m0, s10, 0x22000\n"
    "s_add_u32 s31, s77, 0x14000\n"
    "buffer_load_dwordx4 v2, s[48:51], s31 offen lds\n"
    "buffer_load_dwordx4 v3, s[48:51], s31 offen offset:1024 lds\n"
    "v_mfma_f32_16x16x32_bf16 v[196:199], a[228:231], v[88:91], v[196:199]\n"
    "ds_read_b64_tr_b16 a[200:201], v9 offset:7296\n"
    "v_mfma_f32_16x16x32_bf16 v[224:227], a[228:231], v[92:95], v[224:227]\n"
    "ds_read_b64_tr_b16 a[202:203], v9 offset:10880\n"
    "s_waitcnt vmcnt(10)\n"
    "ds_read_b64_tr_b16 a[130:131], v4 offset:32768\n"
    "ds_read_b64_tr_b16 a[134:135], v5 offset:32768\n"
    "ds_read_b64_tr_b16 a[138:139], v6 offset:32768\n"
    "ds_read_b64_tr_b16 a[142:143], v7 offset:32768\n"
    "s_waitcnt lgkmcnt(14)\n"
    "v_mfma_f32_16x16x32_bf16 v[192:195], a[232:235], v[96:99], v[192:195]\n"
    "ds_read_b64_tr_b16 a[204:205], v9 offset:7328\n"
    "v_mfma_f32_16x16x32_bf16 v[220:223], a[232:235], v[100:103], v[220:223]\n"
    "ds_read_b64_tr_b16 a[206:207], v9 offset:10912\n"
    "v_mfma_f32_16x16x32_bf16 v[196:199], a[236:239], v[96:99], v[196:199]\n"
    "ds_read_b64_tr_b16 a[208:209], v9 offset:14464\n"
    "v_mfma_f32_16x16x32_bf16 v[224:227], a[236:239], v[100:103], v[224:227]\n"
    "ds_read_b64_tr_b16 a[210:211], v9 offset:18048\n"
    "s_waitcnt lgkmcnt(14)\n"
    "v_mfma_f32_16x16x32_bf16 v[192:195], a[240:243], v[104:107], v[192:195]\n"
    "ds_read_b64_tr_b16 a[212:213], v9 offset:14496\n"
    "v_mfma_f32_16x16x32_bf16 v[220:223], a[240:243], v[108:111], v[220:223]\n"
    "ds_read_b64_tr_b16 a[214:215], v9 offset:18080\n"
    "v_mfma_f32_16x16x32_bf16 v[196:199], a[244:247], v[104:107], v[196:199]\n"
    "ds_read_b64_tr_b16 a[216:217], v9 offset:21632\n"
    "v_mfma_f32_16x16x32_bf16 v[224:227], a[244:247], v[108:111], v[224:227]\n"
    "ds_read_b64_tr_b16 a[218:219], v9 offset:25216\n"
    "s_waitcnt lgkmcnt(14)\n"
    "v_mfma_f32_16x16x32_bf16 v[192:195], a[248:251], v[128:131], v[192:195]\n"
    "ds_read_b64_tr_b16 a[220:221], v9 offset:21664\n"
    "v_mfma_f32_16x16x32_bf16 v[220:223], a[248:251], v[132:135], v[220:223]\n"
    "ds_read_b64_tr_b16 a[222:223], v9 offset:25248\n"
    "s_waitcnt lgkmcnt(10)\n"
    "s_add_u32 m0, s10, 0x24000\n"
    "s_add_u32 s31, s78, 0x10000\n"
    "buffer_load_dwordx4 v2, s[48:51], s31 offen lds\n"
    "buffer_load_dwordx4 v3, s[48:51], s31 offen offset:1024 lds\n"
    "v_mfma_f32_16x16x32_bf16 v[196:199], a[252:255], v[128:131], v[196:199]\n"
    "ds_read_b64_tr_b16 a[224:225], v9 offset:28800\n"
    "v_mfma_f32_16x16x32_bf16 v[224:227], a[252:255], v[132:135], v[224:227]\n"
    "ds_read_b64_tr_b16 a[226:227], v9 offset:32384\n"
    "s_waitcnt vmcnt(10)\n"
    "ds_read_b64_tr_b16 a[144:145], v4\n"
    "ds_read_b64_tr_b16 a[148:149], v5\n"
    "ds_read_b64_tr_b16 a[152:153], v6\n"
    "ds_read_b64_tr_b16 a[156:157], v7\n"
    "s_waitcnt lgkmcnt(14)\n"
    "v_mfma_f32_16x16x32_bf16 v[192:195], a[200:203], v[136:139], v[192:195]\n"
    "ds_read_b64_tr_b16 a[228:229], v9 offset:28832\n"
    "v_mfma_f32_16x16x32_bf16 v[220:223], a[200:203], v[140:143], v[220:223]\n"
    "ds_read_b64_tr_b16 a[230:231], v9 offset:32416\n"
    "v_mfma_f32_16x16x32_bf16 v[196:199], a[204:207], v[136:139], v[196:199]\n"
    "ds_read_b64_tr_b16 a[232:233], v9 offset:35968\n"
    "v_mfma_f32_16x16x32_bf16 v[224:227], a[204:207], v[140:143], v[224:227]\n"
    "ds_read_b64_tr_b16 a[234:235], v9 offset:39552\n"
    "s_waitcnt lgkmcnt(14)\n"
    "v_mfma_f32_16x16x32_bf16 v[192:195], a[208:211], v[144:147], v[192:195]\n"
    "ds_read_b64_tr_b16 a[236:237], v9 offset:36000\n"
    "v_mfma_f32_16x16x32_bf16 v[220:223], a[208:211], v[148:151], v[220:223]\n"
    "ds_read_b64_tr_b16 a[238:239], v9 offset:39584\n"
    "v_mfma_f32_16x16x32_bf16 v[196:199], a[212:215], v[144:147], v[196:199]\n"
    "ds_read_b64_tr_b16 a[240:241], v9 offset:43136\n"
    "v_mfma_f32_16x16x32_bf16 v[224:227], a[212:215], v[148:151], v[224:227]\n"
    "ds_read_b64_tr_b16 a[242:243], v9 offset:46720\n"
    "buffer_load_dwordx2 v[26:27], v19, s[56:59], s26 offen sc1\n"
    "s_waitcnt lgkmcnt(14)\n"
    "v_mfma_f32_16x16x32_bf16 v[192:195], a[216:219], v[152:155], v[192:195]\n"
    "ds_read_b64_tr_b16 a[244:245], v9 offset:43168\n"
    "v_mfma_f32_16x16x32_bf16 v[220:223], a[216:219], v[156:159], v[220:223]\n"
    "ds_read_b64_tr_b16 a[246:247], v9 offset:46752\n"
    "v_mfma_f32_16x16x32_bf16 v[196:199], a[220:223], v[152:155], v[196:199]\n"
    "ds_read_b64_tr_b16 a[248:249], v9 offset:50304\n"
    "v_mfma_f32_16x16x32_bf16 v[224:227], a[220:223], v[156:159], v[224:227]\n"
    "ds_read_b64_tr_b16 a[250:251], v9 offset:53888\n"
    "s_waitcnt vmcnt(5)\n"
    "buffer_store_dwordx2 v[24:25], v17, s[56:59], s25 offen offset:1024 sc1\n"
    "s_waitcnt lgkmcnt(10)\n"
    "v_mfma_f32_16x16x32_bf16 v[192:195], a[224:227], v[160:163], v[192:195]\n"
    "ds_read_b64_tr_b16 a[252:253], v9 offset:50336\n"
    "v_mfma_f32_16x16x32_bf16 v[220:223], a[224:227], v[164:167], v[220:223]\n"
    "ds_read_b64_tr_b16 a[254:255], v9 offset:53920\n"
    "s_add_u32 m0, s10, 0x1c000\n"
    "s_add_u32 s31, s78, 0x14000\n"
    "buffer_load_dwordx4 v2, s[48:51], s31 offen lds\n"
    "buffer_load_dwordx4 v3, s[48:51], s31 offen offset:1024 lds\n"
    "v_mfma_f32_16x16x32_bf16 v[196:199], a[228:231], v[160:163], v[196:199]\n"
    "v_mfma_f32_16x16x32_bf16 v[224:227], a[228:231], v[164:167], v[224:227]\n"
    "ds_read_b64_tr_b16 a[146:147], v4 offset:8192\n"
    "ds_read_b64_tr_b16 a[150:151], v5 offset:8192\n"
    "ds_read_b64_tr_b16 a[154:155], v6 offset:8192\n"
    "ds_read_b64_tr_b16 a[158:159], v7 offset:8192\n"
    "s_waitcnt lgkmcnt(12)\n"
    "v_mfma_f32_16x16x32_bf16 v[192:195], a[232:235], v[168:171], v[192:195]\n"
    "v_mfma_f32_16x16x32_bf16 v[220:223], a[232:235], v[172:175], v[220:223]\n"
    "v_mfma_f32_16x16x32_bf16 v[196:199], a[236:239], v[168:171], v[196:199]\n"
    "v_mfma_f32_16x16x32_bf16 v[224:227], a[236:239], v[172:175], v[224:227]\n"
    "s_waitcnt lgkmcnt(8)\n"
    "v_mfma_f32_16x16x32_bf16 v[192:195], a[240:243], v[48:51], v[192:195]\n"
    "v_mfma_f32_16x16x32_bf16 v[220:223], a[240:243], v[52:55], v[220:223]\n"
    "v_mfma_f32_16x16x32_bf16 v[196:199], a[244:247], v[48:51], v[196:199]\n"
    "v_mfma_f32_16x16x32_bf16 v[224:227], a[244:247], v[52:55], v[224:227]\n"
    "s_waitcnt lgkmcnt(0)\n"
    "s_add_u32 m0, s10, 0x1e000\n"
    "s_add_u32 s31, s79, 0x10000\n"
    "buffer_load_dwordx4 v2, s[48:51], s31 offen lds\n"
    "buffer_load_dwordx4 v3, s[48:51], s31 offen offset:1024 lds\n"
    "v_mfma_f32_16x16x32_bf16 v[192:195], a[248:251], v[56:59], v[192:195]\n"
    "v_mfma_f32_16x16x32_bf16 v[220:223], a[248:251], v[60:63], v[220:223]\n"
    "ds_read_b64_tr_b16 a[176:177], v4 offset:16384\n"
    "ds_read_b64_tr_b16 a[180:181], v5 offset:16384\n"
    "ds_read_b64_tr_b16 a[184:185], v6 offset:16384\n"
    "ds_read_b64_tr_b16 a[188:189], v7 offset:16384\n"
    "v_mfma_f32_16x16x32_bf16 v[196:199], a[252:255], v[56:59], v[196:199]\n"
    "v_mfma_f32_16x16x32_bf16 v[224:227], a[252:255], v[60:63], v[224:227]\n"
    "s_nop 0\n"
    "v_pk_fma_f32 v[192:193], v[232:233], v[248:249], v[192:193] op_sel_hi:[0,1,1] neg_lo:[1,0,0] neg_hi:[1,0,0]\n"
    "v_pk_fma_f32 v[194:195], v[232:233], v[250:251], v[194:195] op_sel_hi:[0,1,1] neg_lo:[1,0,0] neg_hi:[1,0,0]\n"
    "s_nop 3\n"
    "v_pk_fma_f32 v[196:197], v[232:233], v[252:253], v[196:197] op_sel_hi:[0,1,1] neg_lo:[1,0,0] neg_hi:[1,0,0]\n"
    "v_pk_fma_f32 v[198:199], v[232:233], v[254:255], v[198:199] op_sel_hi:[0,1,1] neg_lo:[1,0,0] neg_hi:[1,0,0]\n"
    "s_mov_b32 s60, 0x8000\n"
    "s_waitcnt vmcnt(5)\n"
    "L5_%=:\n"
    "v_cmp_ne_u64 vcc, v[26:27], v[24:25]\n"
    "s_and_b32 s88, vcc_lo, 0xff\n"
    "s_cbranch_scc0 L6_%=\n"
    "buffer_load_dwordx2 v[26:27], v19, s[56:59], s26 offen sc1\n"
    "s_sub_u32 s60, s60, 1\n"
    "s_waitcnt vmcnt(0)\n"
    "s_cbranch_scc0 L5_%=\n"
    "L6_%=:\n"
    "buffer_load_dwordx4 v[176:179], v32, s[56:59], s80 offen sc1\n"
    "v_pk_mul_f32 v[240:241], v[192:193], v[192:193]\n"
    "v_pk_mul_f32 v[242:243], v[194:195], v[194:195]\n"
    "v_pk_mul_f32 v[244:245], v[196:197], v[196:197]\n"
    "v_pk_mul_f32 v[246:247], v[198:199], v[198:199]\n"
    "v_pk_fma_f32 v[240:241], v[240:241], v[236:237], v[236:237] op_sel:[0,1,0] op_sel_hi:[1,1,0]\n"
    "v_pk_fma_f32 v[242:243], v[242:243], v[236:237], v[236:237] op_sel:[0,1,0] op_sel_hi:[1,1,0]\n"
    "v_pk_fma_f32 v[244:245], v[244:245], v[236:237], v[236:237] op_sel:[0,1,0] op_sel_hi:[1,1,0]\n"
    "v_pk_fma_f32 v[246:247], v[246:247], v[236:237], v[236:237] op_sel:[0,1,0] op_sel_hi:[1,1,0]\n"
    "buffer_load_dwordx4 v[180:183], v33, s[56:59], s80 offen sc1\n"
    "v_pk_mul_f32 v[240:241], v[240:241], v[192:193]\n"
    "v_pk_mul_f32 v[242:243], v[242:243], v[194:195]\n"
    "v_pk_mul_f32 v[244:245], v[244:245], v[196:197]\n"
    "v_pk_mul_f32 v[246:247], v[246:247], v[198:199]\n"
    "v_exp_f32 v240, v240\n"
    "v_exp_f32 v241, v241\n"
    "v_exp_f32 v242, v242\n"
    "v_exp_f32 v243, v243\n"
    "v_exp_f32 v244, v244\n"
    "v_exp_f32 v245, v245\n"
    "v_exp_f32 v246, v246\n"
    "v_exp_f32 v247, v247\n"
    "s_waitcnt lgkmcnt(0)\n"
    "s_add_u32 m0, s10, 0x20000\n"
    "s_add_u32 s31, s79, 0x14000\n"
    "buffer_load_dwordx4 v2, s[48:51], s31 offen lds\n"
    "buffer_load_dwordx4 v3, s[48:51], s31 offen offset:1024 lds\n"
    "v_pk_fma_f32 v[240:241], v[240:241], v[232:233], v[232:233] op_sel:[0,1,1] op_sel_hi:[1,1,1]\n"
    "v_pk_fma_f32 v[242:243], v[242:243], v[232:233], v[232:233] op_sel:[0,1,1] op_sel_hi:[1,1,1]\n"
    "v_pk_fma_f32 v[244:245], v[244:245], v[232:233], v[232:233] op_sel:[0,1,1] op_sel_hi:[1,1,1]\n"
    "v_pk_fma_f32 v[246:247], v[246:247], v[232:233], v[232:233] op_sel:[0,1,1] op_sel_hi:[1,1,1]\n"
    "buffer_load_dwordx4 v[204:207], v34, s[56:59], s80 offen sc1\n"
    "v_rcp_f32 v240, v240\n"
    "v_rcp_f32 v241, v241\n"
    "v_rcp_f32 v242, v242\n"
    "v_rcp_f32 v243, v243\n"
    "v_rcp_f32 v244, v244\n"
    "v_rcp_f32 v245, v245\n"
    "v_rcp_f32 v246, v246\n"
    "v_rcp_f32 v247, v247\n"
    "ds_read_b64_tr_b16 a[178:179], v4 offset:24576\n"
    "ds_read_b64_tr_b16 a[182:183], v5 offset:24576\n"
    "ds_read_b64_tr_b16 a[186:187], v6 offset:24576\n"
    "ds_read_b64_tr_b16 a[190:191], v7 offset:24576\n"
    "v_pk_mul_f32 v[192:193], v[192:193], v[240:241]\n"
    "v_pk_mul_f32 v[194:195], v[194:195], v[242:243]\n"
    "v_pk_mul_f32 v[196:197], v[196:197], v[244:245]\n"
    "v_pk_mul_f32 v[198:199], v[198:199], v[246:247]\n"
    "buffer_load_dwordx4 v[208:211], v35, s[56:59], s80 offen sc1\n"
    "v_cvt_pk_bf16_f32 v192, v192, v193\n"
    "v_cvt_pk_bf16_f32 v193, v194, v195\n"
    "v_cvt_pk_bf16_f32 v194, v196, v197\n"
    "v_cvt_pk_bf16_f32 v195, v198, v199\n"
    "buffer_store_dwordx4 v[192:195], v1, s[56:59], s91 offen sc1\n"
    "buffer_load_dwordx4 v[184:187], v32, s[56:59], s81 offen sc1\n"
    "v_pk_fma_f32 v[220:221], v[234:235], v[248:249], v[220:221] op_sel_hi:[0,1,1] neg_lo:[1,0,0] neg_hi:[1,0,0]\n"
    "v_pk_fma_f32 v[222:223], v[234:235], v[250:251], v[222:223] op_sel_hi:[0,1,1] neg_lo:[1,0,0] neg_hi:[1,0,0]\n"
    "v_pk_fma_f32 v[224:225], v[234:235], v[252:253], v[224:225] op_sel_hi:[0,1,1] neg_lo:[1,0,0] neg_hi:[1,0,0]\n"
    "v_pk_fma_f32 v[226:227], v[234:235], v[254:255], v[226:227] op_sel_hi:[0,1,1] neg_lo:[1,0,0] neg_hi:[1,0,0]\n"
    "v_pk_mul_f32 v[240:241], v[220:221], v[220:221]\n"
    "v_pk_mul_f32 v[242:243], v[222:223], v[222:223]\n"
    "v_pk_mul_f32 v[244:245], v[224:225], v[224:225]\n"
    "v_pk_mul_f32 v[246:247], v[226:227], v[226:227]\n"
    "buffer_load_dwordx4 v[188:191], v33, s[56:59], s81 offen sc1\n"
    "v_pk_fma_f32 v[240:241], v[240:241], v[238:239], v[238:239] op_sel:[0,1,0] op_sel_hi:[1,1,0]\n"
    "v_pk_fma_f32 v[242:243], v[242:243], v[238:239], v[238:239] op_sel:[0,1,0] op_sel_hi:[1,1,0]\n"
    "v_pk_fma_f32 v[244:245], v[244:245], v[238:239], v[238:239] op_sel:[0,1,0] op_sel_hi:[1,1,0]\n"
    "v_pk_fma_f32 v[246:247], v[246:247], v[238:239], v[238:239] op_sel:[0,1,0] op_sel_hi:[1,1,0]\n"
    "v_pk_mul_f32 v[240:241], v[240:241], v[220:221]\n"
    "v_pk_mul_f32 v[242:243], v[242:243], v[222:223]\n"
    "v_pk_mul_f32 v[244:245], v[244:245], v[224:225]\n"
    "v_pk_mul_f32 v[246:247], v[246:247], v[226:227]\n"
    "v_exp_f32 v240, v240\n"
    "v_exp_f32 v241, v241\n"
    "v_exp_f32 v242, v242\n"
    "v_exp_f32 v243, v243\n"
    "v_exp_f32 v244, v244\n"
    "v_exp_f32 v245, v245\n"
    "v_exp_f32 v246, v246\n"
    "v_exp_f32 v247, v247\n"
    "v_pk_fma_f32 v[240:241], v[240:241], v[234:235], v[234:235] op_sel:[0,1,1] op_sel_hi:[1,1,1]\n"
    "v_pk_fma_f32 v[242:243], v[242:243], v[234:235], v[234:235] op_sel:[0,1,1] op_sel_hi:[1,1,1]\n"
    "v_pk_fma_f32 v[244:245], v[244:245], v[234:235], v[234:235] op_sel:[0,1,1] op_sel_hi:[1,1,1]\n"
    "v_pk_fma_f32 v[246:247], v[246:247], v[234:235], v[234:235] op_sel:[0,1,1] op_sel_hi:[1,1,1]\n"
    "v_rcp_f32 v240, v240\n"
    "v_rcp_f32 v241, v241\n"
    "v_rcp_f32 v242, v242\n"
    "v_rcp_f32 v243, v243\n"
    "v_rcp_f32 v244, v244\n"
    "v_rcp_f32 v245, v245\n"
    "v_rcp_f32 v246, v246\n"
    "v_rcp_f32 v247, v247\n"
    "v_pk_mul_f32 v[220:221], v[220:221], v[240:241]\n"
    "v_pk_mul_f32 v[222:223], v[222:223], v[242:243]\n"
    "v_pk_mul_f32 v[224:225], v[224:225], v[244:245]\n"
    "v_pk_mul_f32 v[226:227], v[226:227], v[246:247]\n"
    "v_cvt_pk_bf16_f32 v220, v220, v221\n"
    "v_cvt_pk_bf16_f32 v221, v222, v223\n"
    "v_cvt_pk_bf16_f32 v222, v224, v225\n"
    "v_cvt_pk_bf16_f32 v223, v226, v227\n"
    "buffer_store_dwordx4 v[220:223], v1, s[56:59], s91 offen offset:1024 sc1\n"
    "ds_read_b64_tr_b16 a[192:193], v4 offset:32768\n"
    "ds_read_b64_tr_b16 a[196:197], v5 offset:32768\n"
    "ds_read_b64_tr_b16 a[200:201], v6 offset:32768\n"
    "ds_read_b64_tr_b16 a[204:205], v7 offset:32768\n"
    "s_waitcnt vmcnt(12)\n"
    "ds_read_b64_tr_b16 a[194:195], v4\n"
    "ds_read_b64_tr_b16 a[198:199], v5\n"
    "ds_read_b64_tr_b16 a[202:203], v6\n"
    "ds_read_b64_tr_b16 a[206:207], v7\n"
    "s_barrier\n"
    "buffer_load_dwordx4 v[212:215], v34, s[56:59], s81 offen sc1\n"
    "buffer_load_dwordx4 v[216:219], v35, s[56:59], s81 offen sc1\n"
    "buffer_load_dwordx4 v[48:51], v32, s[56:59], s82 offen sc1\n"
    "buffer_load_dwordx4 v[52:55], v33, s[56:59], s82 offen sc1\n"
    "buffer_load_dwordx4 v[56:59], v34, s[56:59], s82 offen sc1\n"
    "buffer_load_dwordx4 v[60:63], v35, s[56:59], s82 offen sc1\n"
    "s_waitcnt vmcnt(15)\n"
    "v_mfma_f32_16x16x32_bf16 v[112:115], a[0:3], v[176:179], v[112:115]\n"
    "v_mfma_f32_16x16x32_bf16 v[116:119], a[4:7], v[176:179], v[116:119]\n"
    "buffer_load_dwordx2 v[26:27], v19, s[56:59], s26 offen offset:1024 sc1\n"
    "buffer_load_dwordx4 v[64:67], v32, s[56:59], s83 offen sc1\n"
    "v_mfma_f32_16x16x32_bf16 v[120:123], a[8:11], v[176:179], v[120:123]\n"
    "v_mfma_f32_16x16x32_bf16 v[124:127], a[12:15], v[176:179], v[124:127]\n"
    "s_waitcnt vmcnt(8)\n"
    "buffer_store_dwordx2 v[24:25], v17, s[56:59], s25 offen offset:2048 sc1\n"
    "v_mfma_f32_16x16x32_bf16 v[128:131], a[0:3], v[180:183], 0\n"
    "v_mfma_f32_16x16x32_bf16 v[132:135], a[4:7], v[180:183], 0\n"
    "buffer_load_dwordx4 v[68:71], v33, s[56:59], s83 offen sc1\n"
    "v_mfma_f32_16x16x32_bf16 v[136:139], a[8:11], v[180:183], 0\n"
    "v_mfma_f32_16x16x32_bf16 v[140:143], a[12:15], v[180:183], 0\n"
    "ds_read_b64_tr_b16 a[208:209], v4 offset:8192\n"
    "ds_read_b64_tr_b16 a[212:213], v5 offset:8192\n"
    "ds_read_b64_tr_b16 a[216:217], v6 offset:8192\n"
    "ds_read_b64_tr_b16 a[220:221], v7 offset:8192\n"
    "v_mfma_f32_16x16x32_bf16 v[144:147], a[0:3], v[204:207], 0\n"
    "v_mfma_f32_16x16x32_bf16 v[148:151], a[4:7], v[204:207], 0\n"
    "buffer_load_dwordx4 v[72:75], v34, s[56:59], s83 offen sc1\n"
    "v_mfma_f32_16x16x32_bf16 v[152:155], a[8:11], v[204:207], 0\n"
    "v_mfma_f32_16x16x32_bf16 v[156:159], a[12:15], v[204:207], 0\n"
    "v_mfma_f32_16x16x32_bf16 v[160:163], a[0:3], v[208:211], 0\n"
    "v_mfma_f32_16x16x32_bf16 v[164:167], a[4:7], v[208:211], 0\n"
    "buffer_load_dwordx4 v[76:79], v35, s[56:59], s83 offen sc1\n"
    "v_mfma_f32_16x16x32_bf16 v[168:171], a[8:11], v[208:211], 0\n"
    "v_mfma_f32_16x16x32_bf16 v[172:175], a[12:15], v[208:211], 0\n"
    "v_mfma_f32_16x16x32_bf16 v[112:115], a[16:19], v[184:187], v[112:115]\n"
    "v_mfma_f32_16x16x32_bf16 v[116:119], a[20:23], v[184:187], v[116:119]\n"
    "s_and_b32 s29, s8, 1\n"
    "s_mul_i32 s30, s29, 0x6c00\n"
    "s_add_u32 s28, s86, 0x1800\n"
    "s_add_u32 s28, s28, s30\n"
    "s_mul_i32 s30, s29, 0x7400\n"
    "s_add_u32 s29, s86, 0x8800\n"
    "s_sub_u32 s29, s29, s30\n"
    "buffer_load_dwordx4 v[80:83], v32, s[56:59], s28 offen sc1\n"
    "v_mfma_f32_16x16x32_bf16 v[120:123], a[24:27], v[184:187], v[120:123]\n"
    "v_mfma_f32_16x16x32_bf16 v[124:127], a[28:31], v[184:187], v[124:127]\n"
    "ds_read_b64_tr_b16 a[210:211], v4 offset:16384\n"
    "ds_read_b64_tr_b16 a[214:215], v5 offset:16384\n"
    "ds_read_b64_tr_b16 a[218:219], v6 offset:16384\n"
    "ds_read_b64_tr_b16 a[222:223], v7 offset:16384\n"
    "v_mfma_f32_16x16x32_bf16 v[128:131], a[16:19], v[188:191], v[128:131]\n"
    "v_mfma_f32_16x16x32_bf16 v[132:135], a[20:23], v[188:191], v[132:135]\n"
    "buffer_load_dwordx4 v[84:87], v32, s[56:59], s29 offen sc1\n"
    "v_mfma_f32_16x16x32_bf16 v[136:139], a[24:27], v[188:191], v[136:139]\n"
    "v_mfma_f32_16x16x32_bf16 v[140:143], a[28:31], v[188:191], v[140:143]\n"
    "s_waitcnt vmcnt(13)\n"
    "v_mfma_f32_16x16x32_bf16 v[144:147], a[16:19], v[212:215], v[144:147]\n"
    "v_mfma_f32_16x16x32_bf16 v[148:151], a[20:23], v[212:215], v[148:151]\n"
    "buffer_load_dwordx4 v[88:91], v34, s[56:59], s28 offen sc1\n"
    "v_mfma_f32_16x16x32_bf16 v[152:155], a[24:27], v[212:215], v[152:155]\n"
    "v_mfma_f32_16x16x32_bf16 v[156:159], a[28:31], v[212:215], v[156:159]\n"
    "s_waitcnt vmcnt(13)\n"
    "v_mfma_f32_16x16x32_bf16 v[160:163], a[16:19], v[216:219], v[160:163]\n"
    "v_mfma_f32_16x16x32_bf16 v[164:167], a[20:23], v[216:219], v[164:167]\n"
    "buffer_load_dwordx4 v[92:95], v34, s[56:59], s29 offen sc1\n"
    "v_mfma_f32_16x16x32_bf16 v[168:171], a[24:27], v[216:219], v[168:171]\n"
    "v_mfma_f32_16x16x32_bf16 v[172:175], a[28:31], v[216:219], v[172:175]\n"
    "s_waitcnt vmcnt(13)\n"
    "v_mfma_f32_16x16x32_bf16 v[112:115], a[32:35], v[48:51], v[112:115]\n"
    "v_mfma_f32_16x16x32_bf16 v[116:119], a[36:39], v[48:51], v[116:119]\n"
    "s_and_b32 s29, s8, 1\n"
    "s_mul_i32 s30, s29, 0x6c00\n"
    "s_add_u32 s28, s87, 0x1800\n"
    "s_add_u32 s28, s28, s30\n"
    "s_mul_i32 s30, s29, 0x7400\n"
    "s_add_u32 s29, s87, 0x8800\n"
    "s_sub_u32 s29, s29, s30\n"
    "buffer_load_dwordx4 v[96:99], v32, s[56:59], s28 offen sc1\n"
    "v_mfma_f32_16x16x32_bf16 v[120:123], a[40:43], v[48:51], v[120:123]\n"
    "v_mfma_f32_16x16x32_bf16 v[124:127], a[44:47], v[48:51], v[124:127]\n"
    "s_waitcnt vmcnt(13)\n"
    "v_mfma_f32_16x16x32_bf16 v[128:131], a[32:35], v[52:55], v[128:131]\n"
    "v_mfma_f32_16x16x32_bf16 v[132:135], a[36:39], v[52:55], v[132:135]\n"
    "buffer_load_dwordx4 v[100:103], v32, s[56:59], s29 offen sc1\n"
    "v_mfma_f32_16x16x32_bf16 v[136:139], a[40:43], v[52:55], v[136:139]\n"
    "v_mfma_f32_16x16x32_bf16 v[140:143], a[44:47], v[52:55], v[140:143]\n"
    "s_waitcnt vmcnt(13)\n"
    "v_mfma_f32_16x16x32_bf16 v[144:147], a[32:35], v[56:59], v[144:147]\n"
    "v_mfma_f32_16x16x32_bf16 v[148:151], a[36:39], v[56:59], v[148:151]\n"
    "buffer_load_dwordx4 v[104:107], v34, s[56:59], s28 offen sc1\n"
    "v_mfma_f32_16x16x32_bf16 v[152:155], a[40:43], v[56:59], v[152:155]\n"
    "v_mfma_f32_16x16x32_bf16 v[156:159], a[44:47], v[56:59], v[156:159]\n"
    "s_waitcnt vmcnt(13)\n"
    "v_mfma_f32_16x16x32_bf16 v[160:163], a[32:35], v[60:63], v[160:163]\n"
    "v_mfma_f32_16x16x32_bf16 v[164:167], a[36:39], v[60:63], v[164:167]\n"
    "buffer_load_dwordx4 v[108:111], v34, s[56:59], s29 offen sc1\n"
    "v_mfma_f32_16x16x32_bf16 v[168:171], a[40:43], v[60:63], v[168:171]\n"
    "v_mfma_f32_16x16x32_bf16 v[172:175], a[44:47], v[60:63], v[172:175]\n"
    "s_waitcnt vmcnt(12)\n"
    "v_mfma_f32_16x16x32_bf16 v[112:115], a[48:51], v[64:67], v[112:115]\n"
    "v_mfma_f32_16x16x32_bf16 v[116:119], a[52:55], v[64:67], v[116:119]\n"
    "s_mov_b32 s60, 0x8000\n"
    "L7_%=:\n"
    "v_cmp_ne_u64 vcc, v[26:27], v[24:25]\n"
    "s_and_b32 s88, vcc_lo, 0xff\n"
    "s_cbranch_scc0 L8_%=\n"
    "buffer_load_dwordx2 v[26:27], v19, s[56:59], s26 offen offset:1024 sc1\n"
    "s_sub_u32 s60, s60, 1\n"
    "s_waitcnt vmcnt(0)\n"
    "s_cbranch_scc0 L7_%=\n"
    "L8_%=:\n"
    "s_add_u32 s28, s80, 0x800\n"
    "buffer_load_dwordx4 v[192:195], v32, s[56:59], s28 offen sc1\n"
    "v_mfma_f32_16x16x32_bf16 v[120:123], a[56:59], v[64:67], v[120:123]\n"
    "v_mfma_f32_16x16x32_bf16 v[124:127], a[60:63], v[64:67], v[124:127]\n"
    "s_waitcnt vmcnt(11)\n"
    "v_mfma_f32_16x16x32_bf16 v[128:131], a[48:51], v[68:71], v[128:131]\n"
    "v_mfma_f32_16x16x32_bf16 v[132:135], a[52:55], v[68:71], v[132:135]\n"
    "buffer_load_dwordx4 v[196:199], v33, s[56:59], s28 offen sc1\n"
    "v_mfma_f32_16x16x32_bf16 v[136:139], a[56:59], v[68:71], v[136:139]\n"
    "v_mfma_f32_16x16x32_bf16 v[140:143], a[60:63], v[68:71], v[140:143]\n"
    "s_waitcnt vmcnt(11)\n"
    "v_mfma_f32_16x16x32_bf16 v[144:147], a[48:51], v[72:75], v[144:147]\n"
    "v_mfma_f32_16x16x32_bf16 v[148:151], a[52:55], v[72:75], v[148:151]\n"
    "buffer_load_dwordx4 v[220:223], v34, s[56:59], s28 offen sc1\n"
    "v_mfma_f32_16x16x32_bf16 v[152:155], a[56:59], v[72:75], v[152:155]\n"
    "v_mfma_f32_16x16x32_bf16 v[156:159], a[60:63], v[72:75], v[156:159]\n"
    "s_waitcnt vmcnt(11)\n"
    "v_mfma_f32_16x16x32_bf16 v[160:163], a[48:51], v[76:79], v[160:163]\n"
    "v_mfma_f32_16x16x32_bf16 v[164:167], a[52:55], v[76:79], v[164:167]\n"
    "buffer_load_dwordx4 v[224:227], v35, s[56:59], s28 offen sc1\n"
    "v_mfma_f32_16x16x32_bf16 v[168:171], a[56:59], v[76:79], v[168:171]\n"
    "v_mfma_f32_16x16x32_bf16 v[172:175], a[60:63], v[76:79], v[172:175]\n"
    "s_waitcnt vmcnt(8)\n"
    "s_bitcmp1_b32 s8, 0\n"
    "s_cbranch_scc1 L9_%=\n"
    "v_swap_b32 v82, v84\n"
    "v_swap_b32 v83, v85\n"
    "v_swap_b32 v90, v92\n"
    "v_swap_b32 v91, v93\n"
    "s_branch L10_%=\n"
    "L9_%=:\n"
    "v_swap_b32 v80, v86\n"
    "v_swap_b32 v81, v87\n"
    "v_swap_b32 v88, v94\n"
    "v_swap_b32 v89, v95\n"
    "L10_%=:\n"
    "v_mfma_f32_16x16x32_bf16 v[112:115], a[64:67], v[80:83], v[112:115]\n"
    "v_mfma_f32_16x16x32_bf16 v[116:119], a[68:71], v[80:83], v[116:119]\n"
    "s_add_u32 s28, s81, 0x800\n"
    "buffer_load_dwordx4 v[200:203], v32, s[56:59], s28 offen sc1\n"
    "v_mfma_f32_16x16x32_bf16 v[120:123], a[72:75], v[80:83], v[120:123]\n"
    "v_mfma_f32_16x16x32_bf16 v[124:127], a[76:79], v[80:83], v[124:127]\n"
    "v_mfma_f32_16x16x32_bf16 v[128:131], a[64:67], v[84:87], v[128:131]\n"
    "v_mfma_f32_16x16x32_bf16 v[132:135], a[68:71], v[84:87], v[132:135]\n"
    "buffer_load_dwordx4 v[228:231], v33, s[56:59], s28 offen sc1\n"
    "v_mfma_f32_16x16x32_bf16 v[136:139], a[72:75], v[84:87], v[136:139]\n"
    "v_mfma_f32_16x16x32_bf16 v[140:143], a[76:79], v[84:87], v[140:143]\n"
    "v_mfma_f32_16x16x32_bf16 v[144:147], a[64:67], v[88:91], v[144:147]\n"
    "v_mfma_f32_16x16x32_bf16 v[148:151], a[68:71], v[88:91], v[148:151]\n"
    "buffer_load_dwordx4 v[232:235], v34, s[56:59], s28 offen sc1\n"
    "v_mfma_f32_16x16x32_bf16 v[152:155], a[72:75], v[88:91], v[152:155]\n"
    "v_mfma_f32_16x16x32_bf16 v[156:159], a[76:79], v[88:91], v[156:159]\n"
    "v_mfma_f32_16x16x32_bf16 v[160:163], a[64:67], v[92:95], v[160:163]\n"
    "v_mfma_f32_16x16x32_bf16 v[164:167], a[68:71], v[92:95], v[164:167]\n"
    "buffer_load_dwordx4 v[236:239], v35, s[56:59], s28 offen sc1\n"
    "v_mfma_f32_16x16x32_bf16 v[168:171], a[72:75], v[92:95], v[168:171]\n"
    "v_mfma_f32_16x16x32_bf16 v[172:175], a[76:79], v[92:95], v[172:175]\n"
    "s_waitcnt vmcnt(8)\n"
    "s_bitcmp1_b32 s8, 0\n"
    "s_cbranch_scc1 L11_%=\n"
    "v_swap_b32 v98, v100\n"
    "v_swap_b32 v99, v101\n"
    "v_swap_b32 v106, v108\n"
    "v_swap_b32 v107, v109\n"
    "s_branch L12_%=\n"
    "L11_%=:\n"
    "v_swap_b32 v96, v102\n"
    "v_swap_b32 v97, v103\n"
    "v_swap_b32 v104, v110\n"
    "v_swap_b32 v105, v111\n"
    "L12_%=:\n"
    "v_mfma_f32_16x16x32_bf16 v[112:115], a[80:83], v[96:99], v[112:115]\n"
    "v_mfma_f32_16x16x32_bf16 v[116:119], a[84:87], v[96:99], v[116:119]\n"
    "buffer_load_dwordx2 v[26:27], v19, s[56:59], s26 offen offset:2048 sc1\n"
    "s_add_u32 s28, s82, 0x800\n"
    "buffer_load_dwordx4 v[176:179], v32, s[56:59], s28 offen sc1\n"
    "v_mfma_f32_16x16x32_bf16 v[120:123], a[88:91], v[96:99], v[120:123]\n"
    "v_mfma_f32_16x16x32_bf16 v[124:127], a[92:95], v[96:99], v[124:127]\n"
    "v_mfma_f32_16x16x32_bf16 v[128:131], a[80:83], v[100:103], v[128:131]\n"
    "v_mfma_f32_16x16x32_bf16 v[132:135], a[84:87], v[100:103], v[132:135]\n"
    "buffer_load_dwordx4 v[180:183], v33, s[56:59], s28 offen sc1\n"
    "v_mfma_f32_16x16x32_bf16 v[136:139], a[88:91], v[100:103], v[136:139]\n"
    "v_mfma_f32_16x16x32_bf16 v[140:143], a[92:95], v[100:103], v[140:143]\n"
    "v_mfma_f32_16x16x32_bf16 v[144:147], a[80:83], v[104:107], v[144:147]\n"
    "v_mfma_f32_16x16x32_bf16 v[148:151], a[84:87], v[104:107], v[148:151]\n"
    "buffer_load_dwordx4 v[204:207], v34, s[56:59], s28 offen sc1\n"
    "v_mfma_f32_16x16x32_bf16 v[152:155], a[88:91], v[104:107], v[152:155]\n"
    "v_mfma_f32_16x16x32_bf16 v[156:159], a[92:95], v[104:107], v[156:159]\n"
    "v_mfma_f32_16x16x32_bf16 v[160:163], a[80:83], v[108:111], v[160:163]\n"
    "v_mfma_f32_16x16x32_bf16 v[164:167], a[84:87], v[108:111], v[164:167]\n"
    "buffer_load_dwordx4 v[208:211], v35, s[56:59], s28 offen sc1\n"
    "v_mfma_f32_16x16x32_bf16 v[168:171], a[88:91], v[108:111], v[168:171]\n"
    "v_mfma_f32_16x16x32_bf16 v[172:175], a[92:95], v[108:111], v[172:175]\n"
    "s_waitcnt vmcnt(12)\n"
    "v_mfma_f32_16x16x32_bf16 v[112:115], a[96:99], v[192:195], v[112:115]\n"
    "v_mfma_f32_16x16x32_bf16 v[116:119], a[100:103], v[192:195], v[116:119]\n"
    "s_add_u32 s28, s83, 0x800\n"
    "buffer_load_dwordx4 v[184:187], v32, s[56:59], s28 offen sc1\n"
    "v_mfma_f32_16x16x32_bf16 v[120:123], a[104:107], v[192:195], v[120:123]\n"
    "v_mfma_f32_16x16x32_bf16 v[124:127], a[108:111], v[192:195], v[124:127]\n"
    "s_waitcnt vmcnt(12)\n"
    "v_mfma_f32_16x16x32_bf16 v[128:131], a[96:99], v[196:199], v[128:131]\n"
    "v_mfma_f32_16x16x32_bf16 v[132:135], a[100:103], v[196:199], v[132:135]\n"
    "buffer_load_dwordx4 v[188:191], v33, s[56:59], s28 offen sc1\n"
    "v_mfma_f32_16x16x32_bf16 v[136:139], a[104:107], v[196:199], v[136:139]\n"
    "v_mfma_f32_16x16x32_bf16 v[140:143], a[108:111], v[196:199], v[140:143]\n"
    "s_waitcnt vmcnt(12)\n"
    "v_mfma_f32_16x16x32_bf16 v[144:147], a[96:99], v[220:223], v[144:147]\n"
    "v_mfma_f32_16x16x32_bf16 v[148:151], a[100:103], v[220:223], v[148:151]\n"
    "buffer_load_dwordx4 v[212:215], v34, s[56:59], s28 offen sc1\n"
    "v_mfma_f32_16x16x32_bf16 v[152:155], a[104:107], v[220:223], v[152:155]\n"
    "v_mfma_f32_16x16x32_bf16 v[156:159], a[108:111], v[220:223], v[156:159]\n"
    "s_waitcnt vmcnt(12)\n"
    "v_mfma_f32_16x16x32_bf16 v[160:163], a[96:99], v[224:227], v[160:163]\n"
    "v_mfma_f32_16x16x32_bf16 v[164:167], a[100:103], v[224:227], v[164:167]\n"
    "buffer_load_dwordx4 v[216:219], v35, s[56:59], s28 offen sc1\n"
    "v_mfma_f32_16x16x32_bf16 v[168:171], a[104:107], v[224:227], v[168:171]\n"
    "v_mfma_f32_16x16x32_bf16 v[172:175], a[108:111], v[224:227], v[172:175]\n"
    "s_waitcnt vmcnt(12)\n"
    "v_mfma_f32_16x16x32_bf16 v[112:115], a[112:115], v[200:203], v[112:115]\n"
    "v_mfma_f32_16x16x32_bf16 v[116:119], a[116:119], v[200:203], v[116:119]\n"
    "s_mov_b32 s60, 0x8000\n"
    "s_waitcnt vmcnt(8)\n"
    "L13_%=:\n"
    "v_cmp_ne_u64 vcc, v[26:27], v[24:25]\n"
    "s_and_b32 s88, vcc_lo, 0xff\n"
    "s_cbranch_scc0 L14_%=\n"
    "buffer_load_dwordx2 v[26:27], v19, s[56:59], s26 offen offset:2048 sc1\n"
    "s_sub_u32 s60, s60, 1\n"
    "s_waitcnt vmcnt(0)\n"
    "s_cbranch_scc0 L13_%=\n"
    "L14_%=:\n"
    "s_add_u32 s28, s80, 0x1000\n"
    "buffer_load_dwordx4 v[48:51], v32, s[56:59], s28 offen sc1\n"
    "v_mfma_f32_16x16x32_bf16 v[120:123], a[120:123], v[200:203], v[120:123]\n"
    "v_mfma_f32_16x16x32_bf16 v[124:127], a[124:127], v[200:203], v[124:127]\n"
    "v_mfma_f32_16x16x32_bf16 v[128:131], a[112:115], v[228:231], v[128:131]\n"
    "v_mfma_f32_16x16x32_bf16 v[132:135], a[116:119], v[228:231], v[132:135]\n"
    "buffer_load_dwordx4 v[52:55], v33, s[56:59], s28 offen sc1\n"
    "v_mfma_f32_16x16x32_bf16 v[136:139], a[120:123], v[228:231], v[136:139]\n"
    "v_mfma_f32_16x16x32_bf16 v[140:143], a[124:127], v[228:231], v[140:143]\n"
    "v_mfma_f32_16x16x32_bf16 v[144:147], a[112:115], v[232:235], v[144:147]\n"
    "v_mfma_f32_16x16x32_bf16 v[148:151], a[116:119], v[232:235], v[148:151]\n"
    "buffer_load_dwordx4 v[56:59], v34, s[56:59], s28 offen sc1\n"
    "v_mfma_f32_16x16x32_bf16 v[152:155], a[120:123], v[232:235], v[152:155]\n"
    "v_mfma_f32_16x16x32_bf16 v[156:159], a[124:127], v[232:235], v[156:159]\n"
    "v_mfma_f32_16x16x32_bf16 v[160:163], a[112:115], v[236:239], v[160:163]\n"
    "v_mfma_f32_16x16x32_bf16 v[164:167], a[116:119], v[236:239], v[164:167]\n"
    "buffer_load_dwordx4 v[60:63], v35, s[56:59], s28 offen sc1\n"
    "v_mfma_f32_16x16x32_bf16 v[168:171], a[120:123], v[236:239], v[168:171]\n"
    "v_mfma_f32_16x16x32_bf16 v[172:175], a[124:127], v[236:239], v[172:175]\n"
    "s_waitcnt vmcnt(11)\n"
    "v_mfma_f32_16x16x32_bf16 v[112:115], a[128:131], v[176:179], v[112:115]\n"
    "v_mfma_f32_16x16x32_bf16 v[116:119], a[132:135], v[176:179], v[116:119]\n"
    "s_add_u32 s28, s81, 0x1000\n"
    "buffer_load_dwordx4 v[64:67], v32, s[56:59], s28 offen sc1\n"
    "v_mfma_f32_16x16x32_bf16 v[120:123], a[136:139], v[176:179], v[120:123]\n"
    "v_mfma_f32_16x16x32_bf16 v[124:127], a[140:143], v[176:179], v[124:127]\n"
    "s_waitcnt vmcnt(11)\n"
    "v_mfma_f32_16x16x32_bf16 v[128:131], a[128:131], v[180:183], v[128:131]\n"
    "v_mfma_f32_16x16x32_bf16 v[132:135], a[132:135], v[180:183], v[132:135]\n"
    "buffer_load_dwordx4 v[68:71], v33, s[56:59], s28 offen sc1\n"
    "v_mfma_f32_16x16x32_bf16 v[136:139], a[136:139], v[180:183], v[136:139]\n"
    "v_mfma_f32_16x16x32_bf16 v[140:143], a[140:143], v[180:183], v[140:143]\n"
    "s_waitcnt vmcnt(11)\n"
    "v_mfma_f32_16x16x32_bf16 v[144:147], a[128:131], v[204:207], v[144:147]\n"
    "v_mfma_f32_16x16x32_bf16 v[148:151], a[132:135], v[204:207], v[148:151]\n"
    "buffer_load_dwordx4 v[72:75], v34, s[56:59], s28 offen sc1\n"
    "v_mfma_f32_16x16x32_bf16 v[152:155], a[136:139], v[204:207], v[152:155]\n"
    "v_mfma_f32_16x16x32_bf16 v[156:159], a[140:143], v[204:207], v[156:159]\n"
    "s_waitcnt vmcnt(11)\n"
    "v_mfma_f32_16x16x32_bf16 v[160:163], a[128:131], v[208:211], v[160:163]\n"
    "v_mfma_f32_16x16x32_bf16 v[164:167], a[132:135], v[208:211], v[164:167]\n"
    "buffer_load_dwordx4 v[76:79], v35, s[56:59], s28 offen sc1\n"
    "v_mfma_f32_16x16x32_bf16 v[168:171], a[136:139], v[208:211], v[168:171]\n"
    "v_mfma_f32_16x16x32_bf16 v[172:175], a[140:143], v[208:211], v[172:175]\n"
    "s_waitcnt vmcnt(11)\n"
    "v_mfma_f32_16x16x32_bf16 v[112:115], a[144:147], v[184:187], v[112:115]\n"
    "v_mfma_f32_16x16x32_bf16 v[116:119], a[148:151], v[184:187], v[116:119]\n"
    "s_add_u32 s28, s82, 0x1000\n"
    "buffer_load_dwordx4 v[80:83], v32, s[56:59], s28 offen sc1\n"
    "v_mfma_f32_16x16x32_bf16 v[120:123], a[152:155], v[184:187], v[120:123]\n"
    "v_mfma_f32_16x16x32_bf16 v[124:127], a[156:159], v[184:187], v[124:127]\n"
    "s_waitcnt vmcnt(11)\n"
    "v_mfma_f32_16x16x32_bf16 v[128:131], a[144:147], v[188:191], v[128:131]\n"
    "v_mfma_f32_16x16x32_bf16 v[132:135], a[148:151], v[188:191], v[132:135]\n"
    "buffer_load_dwordx4 v[84:87], v33, s[56:59], s28 offen sc1\n"
    "v_mfma_f32_16x16x32_bf16 v[136:139], a[152:155], v[188:191], v[136:139]\n"
    "v_mfma_f32_16x16x32_bf16 v[140:143], a[156:159], v[188:191], v[140:143]\n"
    "s_waitcnt vmcnt(11)\n"
    "v_mfma_f32_16x16x32_bf16 v[144:147], a[144:147], v[212:215], v[144:147]\n"
    "v_mfma_f32_16x16x32_bf16 v[148:151], a[148:151], v[212:215], v[148:151]\n"
    "buffer_load_dwordx4 v[88:91], v34, s[56:59], s28 offen sc1\n"
    "v_mfma_f32_16x16x32_bf16 v[152:155], a[152:155], v[212:215], v[152:155]\n"
    "v_mfma_f32_16x16x32_bf16 v[156:159], a[156:159], v[212:215], v[156:159]\n"
    "s_waitcnt vmcnt(11)\n"
    "v_mfma_f32_16x16x32_bf16 v[160:163], a[144:147], v[216:219], v[160:163]\n"
    "v_mfma_f32_16x16x32_bf16 v[164:167], a[148:151], v[216:219], v[164:167]\n"
    "buffer_load_dwordx4 v[92:95], v35, s[56:59], s28 offen sc1\n"
    "v_mfma_f32_16x16x32_bf16 v[168:171], a[152:155], v[216:219], v[168:171]\n"
    "v_mfma_f32_16x16x32_bf16 v[172:175], a[156:159], v[216:219], v[172:175]\n"
    "s_waitcnt vmcnt(11)\n"
    "v_mfma_f32_16x16x32_bf16 v[112:115], a[160:163], v[48:51], v[112:115]\n"
    "v_mfma_f32_16x16x32_bf16 v[116:119], a[164:167], v[48:51], v[116:119]\n"
    "s_add_u32 s28, s83, 0x1000\n"
    "buffer_load_dwordx4 v[96:99], v32, s[56:59], s28 offen sc1\n"
    "v_mfma_f32_16x16x32_bf16 v[120:123], a[168:171], v[48:51], v[120:123]\n"
    "v_mfma_f32_16x16x32_bf16 v[124:127], a[172:175], v[48:51], v[124:127]\n"
    "s_waitcnt vmcnt(11)\n"
    "v_mfma_f32_16x16x32_bf16 v[128:131], a[160:163], v[52:55], v[128:131]\n"
    "v_mfma_f32_16x16x32_bf16 v[132:135], a[164:167], v[52:55], v[132:135]\n"
    "buffer_load_dwordx4 v[100:103], v33, s[56:59], s28 offen sc1\n"
    "v_mfma_f32_16x16x32_bf16 v[136:139], a[168:171], v[52:55], v[136:139]\n"
    "v_mfma_f32_16x16x32_bf16 v[140:143], a[172:175], v[52:55], v[140:143]\n"
    "s_waitcnt vmcnt(11)\n"
    "v_mfma_f32_16x16x32_bf16 v[144:147], a[160:163], v[56:59], v[144:147]\n"
    "v_mfma_f32_16x16x32_bf16 v[148:151], a[164:167], v[56:59], v[148:151]\n"
    "buffer_load_dwordx4 v[104:107], v34, s[56:59], s28 offen sc1\n"
    "v_mfma_f32_16x16x32_bf16 v[152:155], a[168:171], v[56:59], v[152:155]\n"
    "v_mfma_f32_16x16x32_bf16 v[156:159], a[172:175], v[56:59], v[156:159]\n"
    "s_waitcnt vmcnt(11)\n"
    "v_mfma_f32_16x16x32_bf16 v[160:163], a[160:163], v[60:63], v[160:163]\n"
    "v_mfma_f32_16x16x32_bf16 v[164:167], a[164:167], v[60:63], v[164:167]\n"
    "buffer_load_dwordx4 v[108:111], v35, s[56:59], s28 offen sc1\n"
    "v_mfma_f32_16x16x32_bf16 v[168:171], a[168:171], v[60:63], v[168:171]\n"
    "v_mfma_f32_16x16x32_bf16 v[172:175], a[172:175], v[60:63], v[172:175]\n"
    "s_waitcnt lgkmcnt(15)\n"
    "s_waitcnt vmcnt(11)\n"
    "v_mfma_f32_16x16x32_bf16 v[112:115], a[176:179], v[64:67], v[112:115]\n"
    "v_mfma_f32_16x16x32_bf16 v[116:119], a[180:183], v[64:67], v[116:119]\n"
    "v_mfma_f32_16x16x32_bf16 v[120:123], a[184:187], v[64:67], v[120:123]\n"
    "v_mfma_f32_16x16x32_bf16 v[124:127], a[188:191], v[64:67], v[124:127]\n"
    "s_waitcnt vmcnt(10)\n"
    "v_mfma_f32_16x16x32_bf16 v[128:131], a[176:179], v[68:71], v[128:131]\n"
    "v_mfma_f32_16x16x32_bf16 v[132:135], a[180:183], v[68:71], v[132:135]\n"
    "v_mfma_f32_16x16x32_bf16 v[136:139], a[184:187], v[68:71], v[136:139]\n"
    "v_mfma_f32_16x16x32_bf16 v[140:143], a[188:191], v[68:71], v[140:143]\n"
    "s_waitcnt vmcnt(9)\n"
    "v_mfma_f32_16x16x32_bf16 v[144:147], a[176:179], v[72:75], v[144:147]\n"
    "v_mfma_f32_16x16x32_bf16 v[148:151], a[180:183], v[72:75], v[148:151]\n"
    "v_mfma_f32_16x16x32_bf16 v[152:155], a[184:187], v[72:75], v[152:155]\n"
    "v_mfma_f32_16x16x32_bf16 v[156:159], a[188:191], v[72:75], v[156:159]\n"
    "s_waitcnt vmcnt(8)\n"
    "v_mfma_f32_16x16x32_bf16 v[160:163], a[176:179], v[76:79], v[160:163]\n"
    "v_mfma_f32_16x16x32_bf16 v[164:167], a[180:183], v[76:79], v[164:167]\n"
    "v_mfma_f32_16x16x32_bf16 v[168:171], a[184:187], v[76:79], v[168:171]\n"
    "v_mfma_f32_16x16x32_bf16 v[172:175], a[188:191], v[76:79], v[172:175]\n"
    "s_waitcnt lgkmcnt(12)\n"
    "s_waitcnt lgkmcnt(8)\n"
    "s_waitcnt vmcnt(7)\n"
    "v_mfma_f32_16x16x32_bf16 v[112:115], a[192:195], v[80:83], v[112:115]\n"
    "v_mfma_f32_16x16x32_bf16 v[116:119], a[196:199], v[80:83], v[116:119]\n"
    "v_mfma_f32_16x16x32_bf16 v[120:123], a[200:203], v[80:83], v[120:123]\n"
    "v_mfma_f32_16x16x32_bf16 v[124:127], a[204:207], v[80:83], v[124:127]\n"
    "s_waitcnt vmcnt(6)\n"
    "v_mfma_f32_16x16x32_bf16 v[128:131], a[192:195], v[84:87], v[128:131]\n"
    "v_mfma_f32_16x16x32_bf16 v[132:135], a[196:199], v[84:87], v[132:135]\n"
    "v_mfma_f32_16x16x32_bf16 v[136:139], a[200:203], v[84:87], v[136:139]\n"
    "v_mfma_f32_16x16x32_bf16 v[140:143], a[204:207], v[84:87], v[140:143]\n"
    "s_waitcnt vmcnt(5)\n"
    "v_mfma_f32_16x16x32_bf16 v[144:147], a[192:195], v[88:91], v[144:147]\n"
    "v_mfma_f32_16x16x32_bf16 v[148:151], a[196:199], v[88:91], v[148:151]\n"
    "v_mfma_f32_16x16x32_bf16 v[152:155], a[200:203], v[88:91], v[152:155]\n"
    "v_mfma_f32_16x16x32_bf16 v[156:159], a[204:207], v[88:91], v[156:159]\n"
    "s_waitcnt vmcnt(4)\n"
    "v_mfma_f32_16x16x32_bf16 v[160:163], a[192:195], v[92:95], v[160:163]\n"
    "v_mfma_f32_16x16x32_bf16 v[164:167], a[196:199], v[92:95], v[164:167]\n"
    "v_mfma_f32_16x16x32_bf16 v[168:171], a[200:203], v[92:95], v[168:171]\n"
    "v_mfma_f32_16x16x32_bf16 v[172:175], a[204:207], v[92:95], v[172:175]\n"
    "s_waitcnt lgkmcnt(4)\n"
    "s_waitcnt lgkmcnt(0)\n"
    "s_waitcnt vmcnt(2)\n"
    "v_mfma_f32_16x16x32_bf16 v[128:131], a[208:211], v[100:103], v[128:131]\n"
    "v_mfma_f32_16x16x32_bf16 v[132:135], a[212:215], v[100:103], v[132:135]\n"
    "v_mfma_f32_16x16x32_bf16 v[136:139], a[216:219], v[100:103], v[136:139]\n"
    "v_mfma_f32_16x16x32_bf16 v[140:143], a[220:223], v[100:103], v[140:143]\n"
    "s_waitcnt vmcnt(1)\n"
    "v_mfma_f32_16x16x32_bf16 v[144:147], a[208:211], v[104:107], v[144:147]\n"
    "s_nop 2\n"
    "ds_write_b128 v41, v[128:131]\n"
    "v_mfma_f32_16x16x32_bf16 v[148:151], a[212:215], v[104:107], v[148:151]\n"
    "ds_write_b128 v41, v[132:135] offset:1024\n"
    "v_mfma_f32_16x16x32_bf16 v[152:155], a[216:219], v[104:107], v[152:155]\n"
    "ds_write_b128 v41, v[136:139] offset:2048\n"
    "v_mfma_f32_16x16x32_bf16 v[156:159], a[220:223], v[104:107], v[156:159]\n"
    "ds_write_b128 v41, v[140:143] offset:3072\n"
    "s_waitcnt vmcnt(0)\n"
    "v_mfma_f32_16x16x32_bf16 v[160:163], a[208:211], v[108:111], v[160:163]\n"
    "ds_write_b128 v42, v[144:147]\n"
    "v_mfma_f32_16x16x32_bf16 v[164:167], a[212:215], v[108:111], v[164:167]\n"
    "ds_write_b128 v42, v[148:151] offset:1024\n"
    "v_mfma_f32_16x16x32_bf16 v[168:171], a[216:219], v[108:111], v[168:171]\n"
    "ds_write_b128 v42, v[152:155] offset:2048\n"
    "v_mfma_f32_16x16x32_bf16 v[172:175], a[220:223], v[108:111], v[172:175]\n"
    "ds_write_b128 v42, v[156:159] offset:3072\n"
    "v_mfma_f32_16x16x32_bf16 v[112:115], a[208:211], v[96:99], v[112:115]\n"
    "ds_write_b128 v43, v[160:163]\n"
    "v_mfma_f32_16x16x32_bf16 v[116:119], a[212:215], v[96:99], v[116:119]\n"
    "ds_write_b128 v43, v[164:167] offset:1024\n"
    "v_mfma_f32_16x16x32_bf16 v[120:123], a[216:219], v[96:99], v[120:123]\n"
    "ds_write_b128 v43, v[168:171] offset:2048\n"
    "v_mfma_f32_16x16x32_bf16 v[124:127], a[220:223], v[96:99], v[124:127]\n"
    "ds_write_b128 v43, v[172:175] offset:3072\n"
    "s_waitcnt lgkmcnt(0)\n"
    "s_barrier\n"
    "ds_read_b128 v[176:179], v40\n"
    "ds_read_b128 v[192:195], v40 offset:4096\n"
    "ds_read_b128 v[208:211], v40 offset:8192\n"
    "ds_read_b128 v[180:183], v40 offset:1024\n"
    "ds_read_b128 v[196:199], v40 offset:5120\n"
    "ds_read_b128 v[212:215], v40 offset:9216\n"
    "ds_read_b128 v[184:187], v40 offset:2048\n"
    "ds_read_b128 v[200:203], v40 offset:6144\n"
    "ds_read_b128 v[216:219], v40 offset:10240\n"
    "ds_read_b128 v[188:191], v40 offset:3072\n"
    "ds_read_b128 v[204:207], v40 offset:7168\n"
    "ds_read_b128 v[220:223], v40 offset:11264\n"
    "s_waitcnt lgkmcnt(9)\n"
    "v_pk_add_f32 v[176:177], v[176:177], v[192:193]\n"
    "v_pk_add_f32 v[178:179], v[178:179], v[194:195]\n"
    "v_pk_add_f32 v[112:113], v[112:113], v[208:209]\n"
    "v_pk_add_f32 v[114:115], v[114:115], v[210:211]\n"
    "v_pk_add_f32 v[112:113], v[112:113], v[176:177]\n"
    "v_pk_add_f32 v[114:115], v[114:115], v[178:179]\n"
    "v_cvt_pk_bf16_f32 v40, v112, v113\n"
    "v_cvt_pk_bf16_f32 v41, v114, v115\n"
    "s_waitcnt lgkmcnt(6)\n"
    "v_pk_add_f32 v[180:181], v[180:181], v[196:197]\n"
    "v_pk_add_f32 v[182:183], v[182:183], v[198:199]\n"
    "v_pk_add_f32 v[116:117], v[116:117], v[212:213]\n"
    "v_pk_add_f32 v[118:119], v[118:119], v[214:215]\n"
    "v_pk_add_f32 v[116:117], v[116:117], v[180:181]\n"
    "v_pk_add_f32 v[118:119], v[118:119], v[182:183]\n"
    "v_cvt_pk_bf16_f32 v42, v116, v117\n"
    "v_cvt_pk_bf16_f32 v43, v118, v119\n"
    "s_nop 0\n"
    "v_permlane16_swap_b32 v40, v42\n"
    "v_permlane16_swap_b32 v41, v43\n"
    "buffer_store_dwordx4 v[40:43], v22, s[52:55], 0 offen sc1\n"
    "s_waitcnt lgkmcnt(3)\n"
    "v_pk_add_f32 v[184:185], v[184:185], v[200:201]\n"
    "v_pk_add_f32 v[186:187], v[186:187], v[202:203]\n"
    "v_pk_add_f32 v[120:121], v[120:121], v[216:217]\n"
    "v_pk_add_f32 v[122:123], v[122:123], v[218:219]\n"
    "v_pk_add_f32 v[120:121], v[120:121], v[184:185]\n"
    "v_pk_add_f32 v[122:123], v[122:123], v[186:187]\n"
    "v_cvt_pk_bf16_f32 v44, v120, v121\n"
    "v_cvt_pk_bf16_f32 v45, v122, v123\n"
    "s_waitcnt lgkmcnt(0)\n"
    "v_pk_add_f32 v[188:189], v[188:189], v[204:205]\n"
    "v_pk_add_f32 v[190:191], v[190:191], v[206:207]\n"
    "v_pk_add_f32 v[124:125], v[124:125], v[220:221]\n"
    "v_pk_add_f32 v[126:127], v[126:127], v[222:223]\n"
    "v_pk_add_f32 v[124:125], v[124:125], v[188:189]\n"
    "v_pk_add_f32 v[126:127], v[126:127], v[190:191]\n"
    "v_cvt_pk_bf16_f32 v46, v124, v125\n"
    "v_cvt_pk_bf16_f32 v47, v126, v127\n"
    "s_nop 0\n"
    "v_permlane16_swap_b32 v44, v46\n"
    "v_permlane16_swap_b32 v45, v47\n"
    "buffer_store_dwordx4 v[44:47], v22, s[52:55], 0 offen offset:64 sc1\n"
    "s_cmp_lg_u32 s61, 0\n"
    "s_cbranch_scc1 L15_%=\n"
    "s_mov_b32 s28, 0xc0000\n"
    "buffer_store_dwordx2 v[24:25], v17, s[56:59], s28 offen sc1\n"
    "L15_%=:\n"
    "LEND_%=:\n"
    :: "s"(__builtin_amdgcn_kernarg_segment_ptr()), "s"(blockIdx.x), "v"(threadIdx.x)
    : "v1", "v2", "v3", "v4", "v5", "v6", "v7", "v8", "v9", "v10", "v11", "v12", "v13", "v14", "v15", "v16", "v17", "v18", "v19", "v20", "v21", "v22", "v23", "v24", "v25", "v26", "v27", "v28", "v29", "v30", "v31", "v32", "v33", "v34", "v35", "v36", "v37", "v38", "v39", "v40", "v41", "v42", "v43", "v44", "v45", "v46", "v47", "v48", "v49", "v50", "v51", "v52", "v53", "v54", "v55", "v56", "v57", "v58", "v59", "v60", "v61", "v62", "v63", "v64", "v65", "v66", "v67", "v68", "v69", "v70", "v71", "v72", "v73", "v74", "v75", "v76", "v77", "v78", "v79", "v80", "v81", "v82", "v83", "v84", "v85", "v86", "v87", "v88", "v89", "v90", "v91", "v92", "v93", "v94", "v95", "v96", "v97", "v98", "v99", "v100", "v101", "v102", "v103", "v104", "v105", "v106", "v107", "v108", "v109", "v110", "v111", "v112", "v113", "v114", "v115", "v116", "v117", "v118", "v119", "v120", "v121", "v122", "v123", "v124", "v125", "v126", "v127", "v128", "v129", "v130", "v131", "v132", "v133", "v134", "v135", "v136", "v137", "v138", "v139", "v140", "v141", "v142", "v143", "v144", "v145", "v146", "v147", "v148", "v149", "v150", "v151", "v152", "v153", "v154", "v155", "v156", "v157", "v158", "v159", "v160", "v161", "v162", "v163", "v164", "v165", "v166", "v167", "v168", "v169", "v170", "v171", "v172", "v173", "v174", "v175", "v176", "v177", "v178", "v179", "v180", "v181", "v182", "v183", "v184", "v185", "v186", "v187", "v188", "v189", "v190", "v191", "v192", "v193", "v194", "v195", "v196", "v197", "v198", "v199", "v200", "v201", "v202", "v203", "v204", "v205", "v206", "v207", "v208", "v209", "v210", "v211", "v212", "v213", "v214", "v215", "v216", "v217", "v218", "v219", "v220", "v221", "v222", "v223", "v224", "v225", "v226", "v227", "v228", "v229", "v230", "v231", "v232", "v233", "v234", "v235", "v236", "v237", "v238", "v239", "v240", "v241", "v242", "v243", "v244", "v245", "v246", "v247", "v248", "v249", "v250", "v251", "v252", "v253", "v254", "v255", "a0", "a1", "a2", "a3", "a4", "a5", "a6", "a7", "a8", "a9", "a10", "a11", "a12", "a13", "a14", "a15", "a16", "a17", "a18", "a19", "a20", "a21", "a22", "a23", "a24", "a25", "a26", "a27", "a28", "a29", "a30", "a31", "a32", "a33", "a34", "a35", "a36", "a37", "a38", "a39", "a40", "a41", "a42", "a43", "a44", "a45", "a46", "a47", "a48", "a49", "a50", "a51", "a52", "a53", "a54", "a55", "a56", "a57", "a58", "a59", "a60", "a61", "a62", "a63", "a64", "a65", "a66", "a67", "a68", "a69", "a70", "a71", "a72", "a73", "a74", "a75", "a76", "a77", "a78", "a79", "a80", "a81", "a82", "a83", "a84", "a85", "a86", "a87", "a88", "a89", "a90", "a91", "a92", "a93", "a94", "a95", "a96", "a97", "a98", "a99", "a100", "a101", "a102", "a103", "a104", "a105", "a106", "a107", "a108", "a109", "a110", "a111", "a112", "a113", "a114", "a115", "a116", "a117", "a118", "a119", "a120", "a121", "a122", "a123", "a124", "a125", "a126", "a127", "a128", "a129", "a130", "a131", "a132", "a133", "a134", "a135", "a136", "a137", "a138", "a139", "a140", "a141", "a142", "a143", "a144", "a145", "a146", "a147", "a148", "a149", "a150", "a151", "a152", "a153", "a154", "a155", "a156", "a157", "a158", "a159", "a160", "a161", "a162", "a163", "a164", "a165", "a166", "a167", "a168", "a169", "a170", "a171", "a172", "a173", "a174", "a175", "a176", "a177", "a178", "a179", "a180", "a181", "a182", "a183", "a184", "a185", "a186", "a187", "a188", "a189", "a190", "a191", "a192", "a193", "a194", "a195", "a196", "a197", "a198", "a199", "a200", "a201", "a202", "a203", "a204", "a205", "a206", "a207", "a208", "a209", "a210", "a211", "a212", "a213", "a214", "a215", "a216", "a217", "a218", "a219", "a220", "a221", "a222", "a223", "a224", "a225", "a226", "a227", "a228", "a229", "a230", "a231", "a232", "a233", "a234", "a235", "a236", "a237", "a238", "a239", "a240", "a241", "a242", "a243", "a244", "a245", "a246", "a247", "a248", "a249", "a250", "a251", "a252", "a253", "a254", "a255", "s8", "s9", "s10", "s11", "s12", "s13", "s14", "s15", "s16", "s17", "s18", "s19", "s20", "s21", "s22", "s23", "s24", "s25", "s26", "s27", "s28", "s29", "s30", "s31", "s32", "s33", "s34", "s35", "s36", "s37", "s38", "s39", "s40", "s41", "s42", "s43", "s44", "s45", "s46", "s47", "s48", "s49", "s50", "s51", "s52", "s53", "s54", "s55", "s56", "s57", "s58", "s59", "s60", "s61", "s62", "s63", "s64", "s65", "s66", "s67", "s68", "s69", "s70", "s71", "s72", "s73", "s74", "s75", "s76", "s77", "s78", "s79", "s80", "s81", "s82", "s83", "s84", "s85", "s86", "s87", "s88", "s89", "s90", "s91", "vcc", "m0", "scc", "memory");
}
extern "C" void kernel_launch(void* const* d_in, const int* in_sizes, int n_in, void* d_out, int out_size,
                              void* d_ws, size_t ws_size, hipStream_t stream) {
  (void)hipFuncSetAttribute((const void*)mixer, hipFuncAttributeMaxDynamicSharedMemorySize, L_TOTAL);
  mixer<<<256, 256, L_TOTAL, stream>>>(d_in[0], d_in[1], d_in[2], d_in[3], d_in[4], d_out, d_ws);
}
```
